```python
import torch

def _softmax(x, axis=-1):
    return torch.softmax(x, dim=axis)
def _gelu(x):
    return torch.nn.functional.gelu(x, approximate="tanh")
def _silu(x):
    return torch.nn.functional.silu(x)
def _sigmoid(x):
    return torch.sigmoid(x)
def _layernorm(x, axis=-1, eps=1e-5):
    m = x.mean(dim=axis, keepdim=True)
    v = x.var(dim=axis, keepdim=True, unbiased=False)
    return (x - m) / torch.sqrt(v + eps)
def _scatter_add(tgt, idx, src, axis=0):
    return tgt.clone().index_add_(axis, idx, src)
def _segment_sum(src, seg, n):
    out = torch.zeros((n,) + src.shape[1:], dtype=src.dtype)
    return out.index_add_(0, seg, src)
def _pad(x, pw):
    flat = tuple(v for pair in reversed(pw) for v in pair)
    return torch.nn.functional.pad(x, flat)

kernel_name = 'gated_mix__softmax_full_mha__dense_activation_silu__prompt'

def setup_inputs():

    _w = (lambda a: (lambda t: t.to(torch.bfloat16) if t.is_floating_point() else t)(torch.as_tensor(a)))
    def _A_setup():
        B, S, D = 1, 2048, 512
        return {
            "x": _w((lambda *s: torch.randn(*s, dtype=torch.bfloat16) * 0.05)(B, S, D)),
            "wqkv": _w((lambda *s: torch.randn(*s, dtype=torch.bfloat16) * 0.8)(D, 3 * D)),
            "wo": _w((lambda *s: torch.randn(*s, dtype=torch.bfloat16) * 0.05)(D, D)),
        }
    _A_in = _A_setup()

    def _B_setup():
        B, S, D, F = 1, 2048, 512, 1792
        return {
            "x": _w((lambda *s: torch.randn(*s, dtype=torch.bfloat16) * 0.05)(B, S, D)),
            "w1": _w((lambda *s: torch.randn(*s, dtype=torch.bfloat16) * 3.2)(D, F) * (D ** -0.5)),
            "w2": _w((lambda *s: torch.randn(*s, dtype=torch.bfloat16) * 0.8)(F, D) * (F ** -0.5)),
        }
    _B_in = _B_setup()

    return {
        "A_x": _A_in["x"],
        "A_wqkv": _A_in["wqkv"],
        "A_wo": _A_in["wo"],
        "B_x": _B_in["x"],
        "B_w1": _B_in["w1"],
        "B_w2": _B_in["w2"],
        "gate_w": _w((lambda *s: torch.randn(*s, dtype=torch.bfloat16) * 0.05)(512) * 16.0),
    }

def reference(A_x, A_wqkv, A_wo, B_x, B_w1, B_w2, gate_w):
    def _A_ref(x, wqkv, wo):
        B, S, D = x.shape
        H = 32; Dh = D // H
        qkv = (lambda x, *d: x.permute(*d))((x @ wqkv).reshape(B, S, 3, H, Dh), 2, 0, 3, 1, 4)
        q, k, v = qkv[0], qkv[1], qkv[2]
        scale = Dh ** -0.5
        scores = torch.einsum("bhqd,bhkd->bhqk", q, k) * scale
        p = _softmax(scores, axis=-1)
        o = torch.einsum("bhqk,bhkd->bhqd", p, v)
        return (lambda x, *d: x.permute(*d))(o, 0, 2, 1, 3).reshape(B, S, D) @ wo

    def _B_ref(x, w1, w2):
        h = torch.einsum("bsd,df->bsf", x, w1)
        h = _silu(h)
        out = torch.einsum("bsf,fd->bsd", h, w2)
        return out

    x = A_x
    g = _sigmoid((x * gate_w).sum(-1, keepdims=True))
    a = _A_ref(x, A_wqkv, A_wo)
    b = _B_ref(x, B_w1, B_w2)
    return g * a + (1 - g) * b
```

```cpp
#include <hip/hip_runtime.h>
#include <stdint.h>

typedef __bf16 bf16x8 __attribute__((ext_vector_type(8)));
typedef __bf16 bf16x16 __attribute__((ext_vector_type(16)));
typedef __bf16 bf16x2 __attribute__((ext_vector_type(2)));
typedef short s16x4 __attribute__((ext_vector_type(4)));
typedef short s16x8 __attribute__((ext_vector_type(8)));
typedef float f32x2 __attribute__((ext_vector_type(2)));
typedef float f32x4 __attribute__((ext_vector_type(4)));
typedef float f32x8 __attribute__((ext_vector_type(8)));
typedef float f32x16 __attribute__((ext_vector_type(16)));
typedef unsigned u32x4 __attribute__((ext_vector_type(4)));
#define LAS __attribute__((address_space(3)))
#define GAS __attribute__((address_space(1)))
#define DI __device__ __forceinline__
typedef LAS char* ldsp;

constexpr int S_ = 2048, D_ = 512, F_ = 1792;
constexpr int NKS = 144;
constexpr unsigned WS_Q = 0;
constexpr unsigned WS_KV = WS_Q + 32u * 64 * 1024;
constexpr unsigned WS_A = WS_KV + 32u * 64 * 2048;
constexpr unsigned WS_W = WS_A + 64u * NKS * 1024;
constexpr unsigned WS_G = WS_W + 16u * NKS * 1024;
constexpr unsigned WS_END = WS_G + 2048 * 4;
constexpr float QSCALE = 0.25f * 1.4426950408889634f;
constexpr float LOG2E = 1.4426950408889634f;

#define MFMA(a, b, c) __builtin_amdgcn_mfma_f32_32x32x16_bf16((a), (b), (c), 0, 0, 0)
#define WAIT_BAR(N) asm volatile("s_waitcnt vmcnt(" #N ") lgkmcnt(0)\n\ts_barrier" ::: "memory")

DI void glds(unsigned voff, const void* sbase, unsigned lds) {
    asm volatile("s_mov_b32 m0, %2\n\ts_nop 0\n\tglobal_load_lds_dwordx4 %0, %1" :: "v"(voff), "s"(sbase), "s"(lds) : "memory");
}
template <int N> DI void wait_bar() { asm volatile("s_waitcnt vmcnt(%0) lgkmcnt(0)\n\ts_barrier" :: "i"(N) : "memory"); }
DI unsigned pk(float a, float b) { f32x2 v = {a, b}; return __builtin_bit_cast(unsigned, __builtin_convertvector(v, bf16x2)); }
DI u32x4 pk8(const f32x16& a, int o, float s) {
    return (u32x4){pk(a[o] * s, a[o + 1] * s), pk(a[o + 2] * s, a[o + 3] * s), pk(a[o + 4] * s, a[o + 5] * s), pk(a[o + 6] * s, a[o + 7] * s)}; }
DI bf16x8 ldsr(ldsp p) { return *(const LAS bf16x8*)p; }
DI bf16x8 ldtr(ldsp p0, ldsp p1) {
    s16x4 a = __builtin_amdgcn_ds_read_tr16_b64_v4i16((LAS s16x4*)p0), b = __builtin_amdgcn_ds_read_tr16_b64_v4i16((LAS s16x4*)p1);
    return __builtin_bit_cast(bf16x8, __builtin_shufflevector(a, b, 0, 1, 2, 3, 4, 5, 6, 7)); }
DI void st16(__amdgpu_buffer_rsrc_t r, unsigned voff, unsigned soff, u32x4 v) { __builtin_amdgcn_raw_buffer_store_b128(v, r, voff + soff, 0, 0); }
DI bf16x8 ld16(__amdgpu_buffer_rsrc_t r, unsigned voff, unsigned soff) { return __builtin_bit_cast(bf16x8, __builtin_amdgcn_raw_buffer_load_b128(r, voff, soff, 0)); }
DI float silu(float v) { return v * __builtin_amdgcn_rcpf(1.f + __builtin_amdgcn_exp2f(-LOG2E * v)); }

struct P1 {
    const char* xs; const char* w1s; const char* w2s;
    unsigned ld1, ld2; unsigned vx0, vx1, vw1, vw2; unsigned lds0; int rot, w;
};
constexpr int slot_of(int item) { return ((item + 6) % 10) * 16384; }
template <int I> DI void issue_item(const P1& p) {
    if constexpr (I < 24) {
        constexpr int kk = I < 16 ? I / 2 : I - 16;
        const int kc = (kk + p.rot) & 7;
        const unsigned dst = p.lds0 + slot_of(I) + p.w * 2048;
        if constexpr (I < 16 && (I & 1) == 0) { const char* s = p.xs + kc * 128; glds(p.vx0, s, dst); glds(p.vx1, s, dst + 1024); }
        else if constexpr (I < 16) { const char* s = p.w1s + (size_t)(kc * 64) * p.ld1; glds(p.vw1, s, dst); glds(p.vw1, s + 8 * p.ld1, dst + 1024); }
        else { const char* s = p.w2s + (size_t)(kc * 64) * p.ld2; glds(p.vw2, s, dst); glds(p.vw2, s + 8 * p.ld2, dst + 1024); }
    }
}
template <int KK, bool SWAP> DI void pass1_step(const P1& p, ldsp lds, const unsigned (&xa)[4], unsigned wa0, unsigned wa1, bf16x8 (&xf)[32], f32x16& c0, f32x16& c1) {
    wait_bar<(KK == 0 ? 16 : 12)>();
    if constexpr (KK >= 1) { issue_item<8 + 2 * KK>(p); issue_item<9 + 2 * KK>(p); }
    ldsp sx = lds + slot_of(2 * KK), sw = lds + slot_of(2 * KK + 1);
#pragma unroll
    for (int s = 0; s < 4; ++s) {
        xf[KK * 4 + s] = ldsr(sx + xa[s]);
        bf16x8 f0 = ldtr(sw + wa0 + s * 2048, sw + wa0 + s * 2048 + 512), f1 = ldtr(sw + wa1 + s * 2048, sw + wa1 + s * 2048 + 512);
        if constexpr (SWAP) { c0 = MFMA(xf[KK * 4 + s], f0, c0); c1 = MFMA(xf[KK * 4 + s], f1, c1); }
        else { c0 = MFMA(f0, xf[KK * 4 + s], c0); c1 = MFMA(f1, xf[KK * 4 + s], c1); }
    }
}
template <bool SWAP> DI void pass1(const P1& p, ldsp lds, const unsigned (&xa)[4], unsigned wa0, unsigned wa1, bf16x8 (&xf)[32], f32x16& c0, f32x16& c1) {
    pass1_step<0, SWAP>(p, lds, xa, wa0, wa1, xf, c0, c1); pass1_step<1, SWAP>(p, lds, xa, wa0, wa1, xf, c0, c1);
    pass1_step<2, SWAP>(p, lds, xa, wa0, wa1, xf, c0, c1); pass1_step<3, SWAP>(p, lds, xa, wa0, wa1, xf, c0, c1);
    pass1_step<4, SWAP>(p, lds, xa, wa0, wa1, xf, c0, c1); pass1_step<5, SWAP>(p, lds, xa, wa0, wa1, xf, c0, c1);
    pass1_step<6, SWAP>(p, lds, xa, wa0, wa1, xf, c0, c1); pass1_step<7, SWAP>(p, lds, xa, wa0, wa1, xf, c0, c1);
}
template <int KK> DI void pass2_step(ldsp lds, unsigned wa0, unsigned wa1, const bf16x8 (&xf)[32], f32x16& c0, f32x16& c1) {
    ldsp sw = lds + slot_of(16 + KK);
#pragma unroll
    for (int s = 0; s < 4; ++s) {
        bf16x8 f0 = ldtr(sw + wa0 + s * 2048, sw + wa0 + s * 2048 + 512), f1 = ldtr(sw + wa1 + s * 2048, sw + wa1 + s * 2048 + 512);
        c0 = MFMA(f0, xf[KK * 4 + s], c0); c1 = MFMA(f1, xf[KK * 4 + s], c1);
    }
}
DI void store_h(__amdgpu_buffer_rsrc_t ws, f32x16 c, int rowtile, int ftile, int lane) {
#pragma unroll
    for (int i = 0; i < 16; ++i) c[i] = silu(c[i]);
    const unsigned so = WS_A + (unsigned)(rowtile * NKS + 32 + 2 * ftile) * 1024;
    st16(ws, lane * 16, so, pk8(c, 0, 1.f)); st16(ws, lane * 16, so + 1024, pk8(c, 8, 1.f));
}

__global__ void __launch_bounds__(512, 2) k_phase1(const char* x, const char* wqkv, const char* wo, const char* w1, const char* w2, const char* gw, char* wsp) {
    extern __shared__ __attribute__((aligned(16))) char smem[];
    ldsp lds = (ldsp)smem;
    const int lane = threadIdx.x & 63, w = __builtin_amdgcn_readfirstlane(threadIdx.x >> 6);
    const int xcd = blockIdx.x & 7, j = blockIdx.x >> 3;
    const int rb = (xcd >> 1) * 4 + (j >> 3), cb = (xcd & 1) * 8 + (j & 7);
    const bool isqkv = cb < 12;
    const __amdgpu_buffer_rsrc_t ws = __builtin_amdgcn_make_buffer_rsrc(wsp, 0, WS_END, 0x00020000);
    P1 p;
    p.w = w; p.rot = (rb + cb) & 7; p.lds0 = (unsigned)(uintptr_t)lds;
    p.ld1 = isqkv ? 3072 : 3584; p.ld2 = 3584;
    const int win2 = isqkv ? ((512 + 96 * cb) & ~63) : 1664;
    const int mask2 = isqkv ? ((cb & 1) ? 14 : 7) : (1 << (cb - 12));
    p.xs = x + (size_t)(rb * 128 + 16 * w) * 1024;
    p.w1s = (isqkv ? wqkv + cb * 256 : w1 + (cb - 12) * 256) + (size_t)(16 * (w & 3)) * p.ld1 + (w >> 2) * 128;
    p.w2s = w1 + win2 * 2 + (size_t)(16 * (w & 3)) * p.ld2 + (w >> 2) * 128;
    p.vx0 = (lane >> 3) * 1024 + (((lane & 7) ^ (lane >> 4)) << 4);
    p.vx1 = ((lane >> 3) + 8) * 1024 + (((lane & 7) ^ (4 + (lane >> 4))) << 4);
    const unsigned wsw = ((lane & 7) ^ (((lane >> 4) & 1) << 2)) << 4;
    p.vw1 = (lane >> 3) * p.ld1 + wsw; p.vw2 = (lane >> 3) * p.ld2 + wsw;
    issue_item<0>(p); issue_item<1>(p); issue_item<2>(p); issue_item<3>(p); issue_item<4>(p);
    issue_item<5>(p); issue_item<6>(p); issue_item<7>(p); issue_item<8>(p); issue_item<9>(p);

    const int rt = w & 3, ch = w >> 2, r = lane & 31, h = lane >> 5;
    unsigned xa[4];
#pragma unroll
    for (int s = 0; s < 4; ++s) xa[s] = (rt * 32 + r) * 128 + (((2 * s + h) ^ ((r >> 1) & 7)) << 4);
    const int q4 = (lane & 15) >> 2, p4 = lane & 3, blk = (lane >> 4) & 1;
    const unsigned wa0 = ch * 8192 + (8 * h + q4) * 128 + ((blk * 32 + p4 * 8) ^ ((q4 >> 1) << 6)), wa1 = wa0 ^ 64;
    bf16x8 xf[32]; f32x16 c0 = {}, c1 = {};
    const int rowtile = rb * 4 + rt;
    if (cb >= 8 && cb < 12) pass1<true>(p, lds, xa, wa0, wa1, xf, c0, c1); else pass1<false>(p, lds, xa, wa0, wa1, xf, c0, c1);
    {
        const int ct = (cb & 3) * 4 + ch * 2;
        if (cb < 4) {
            const unsigned so = WS_Q + (unsigned)(ct * 2 * 64 + rowtile) * 1024;
            st16(ws, lane * 16, so, pk8(c0, 0, QSCALE)); st16(ws, lane * 16, so + 65536, pk8(c0, 8, QSCALE));
            st16(ws, lane * 16, so + 2 * 65536, pk8(c1, 0, QSCALE)); st16(ws, lane * 16, so + 3 * 65536, pk8(c1, 8, QSCALE));
        } else if (cb < 8) {
            const unsigned lp = ((lane & 0x33) | ((lane & 4) << 1) | ((lane & 8) >> 1)) * 16;
            const unsigned so = WS_KV + (unsigned)(ct * 2 * 64 + rowtile) * 2048;
            st16(ws, lp, so, pk8(c0, 0, 1.f)); st16(ws, lp, so + 131072, pk8(c0, 8, 1.f));
            st16(ws, lp, so + 2 * 131072, pk8(c1, 0, 1.f)); st16(ws, lp, so + 3 * 131072, pk8(c1, 8, 1.f));
        } else if (cb < 12) {
            const unsigned lp = blk * 131072 + h * 256 + (lane & 15) * 16;
            const unsigned so = WS_KV + (unsigned)(ct * 2 * 64 + rowtile) * 2048 + 1024;
            st16(ws, lp, so, pk8(c0, 0, 1.f)); st16(ws, lp, so + 512, pk8(c0, 8, 1.f));
            st16(ws, lp, so + 2 * 131072, pk8(c1, 0, 1.f)); st16(ws, lp, so + 2 * 131072 + 512, pk8(c1, 8, 1.f));
        } else {
            store_h(ws, c0, rowtile, ct, lane); store_h(ws, c1, rowtile, ct + 1, lane);
        }
    }
    c0 = f32x16{}; c1 = f32x16{};
    wait_bar<0>();
    pass2_step<0>(lds, wa0, wa1, xf, c0, c1); pass2_step<1>(lds, wa0, wa1, xf, c0, c1); pass2_step<2>(lds, wa0, wa1, xf, c0, c1); pass2_step<3>(lds, wa0, wa1, xf, c0, c1);
    pass2_step<4>(lds, wa0, wa1, xf, c0, c1); pass2_step<5>(lds, wa0, wa1, xf, c0, c1); pass2_step<6>(lds, wa0, wa1, xf, c0, c1); pass2_step<7>(lds, wa0, wa1, xf, c0, c1);
    {
        const int ft = (win2 >> 5) + ch * 2;
        if ((mask2 >> (2 * ch)) & 1) store_h(ws, c0, rowtile, ft, lane);
        if ((mask2 >> (2 * ch + 1)) & 1) store_h(ws, c1, rowtile, ft + 1, lane);
    }
    if (!isqkv) {
        if (cb == 12 && ch == 1) {
            f32x16 g = {};
#pragma unroll
            for (int k = 0; k < 32; ++k) { const int kc = ((k >> 2) + p.rot) & 7;
                bf16x8 gf = *(const GAS bf16x8*)(gw + kc * 128 + (k & 3) * 32 + h * 16); g = MFMA(gf, xf[k], g); }
            const float gv = __builtin_amdgcn_rcpf(1.f + __builtin_amdgcn_exp2f(-LOG2E * g[0]));
            if (h == 0) __builtin_amdgcn_raw_buffer_store_b32(__builtin_bit_cast(unsigned, gv), ws, r * 4, WS_G + rowtile * 128, 0);
        }
        const int wg = (rb * 4 + (cb - 12)) * 8 + w;
        const int nl = 16 * ((r >> 2) & 1) + (r & 3) + 4 * (r >> 3);
        for (int f = wg; f < 16 * NKS; f += 512) {
            const int ks = f >> 4, ct = f & 15;
            const GAS unsigned short* src = (const GAS unsigned short*)(ks < 32 ? wo + (size_t)ks * 16 * 1024 : w2 + (size_t)(ks - 32) * 16 * 1024) + ct * 32 + nl + 4 * h * 512;
            u32x4 v;
            v[0] = src[0] | ((unsigned)src[512] << 16); v[1] = src[2 * 512] | ((unsigned)src[3 * 512] << 16);
            v[2] = src[8 * 512] | ((unsigned)src[9 * 512] << 16); v[3] = src[10 * 512] | ((unsigned)src[11 * 512] << 16);
            st16(ws, lane * 16, WS_W + (unsigned)(ct * NKS + ks) * 1024, v);
        }
    }
}

DI float halfmax(float m) { auto rr = __builtin_amdgcn_permlane32_swap(__float_as_uint(m), __float_as_uint(m), false, false); return fmaxf(__uint_as_float(rr[0]), __uint_as_float(rr[1])); }
DI float max16(const f32x16& s) { float m = fmaxf(fmaxf(s[0], s[1]), s[2]);
#pragma unroll
    for (int i = 3; i < 15; i += 2) m = fmaxf(fmaxf(m, s[i]), s[i + 1]);
    return fmaxf(m, s[15]); }
DI f32x16 pv(bf16x8 v, const f32x16& p, int o, f32x16 acc, int idx) {
    f32x8 b = o ? __builtin_shufflevector(p, p, 8, 9, 10, 11, 12, 13, 14, 15) : __builtin_shufflevector(p, p, 0, 1, 2, 3, 4, 5, 6, 7);
    return __builtin_amdgcn_smfmac_f32_32x32x32_bf16(v, __builtin_bit_cast(bf16x16, b), acc, idx, 0, 0); }
DI void exp16(f32x16& s) {
#pragma unroll
    for (int i = 0; i < 16; ++i) s[i] = __builtin_amdgcn_exp2f(s[i]); }

constexpr unsigned LDS_ONES = 131072;
__global__ void __launch_bounds__(512, 2) k_attn(char* wsp) {
    extern __shared__ __attribute__((aligned(16))) char smem[];
    ldsp lds = (ldsp)smem;
    const int lane = threadIdx.x & 63, w = __builtin_amdgcn_readfirstlane(threadIdx.x >> 6);
    const int xcd = blockIdx.x & 7, j = blockIdx.x >> 3;
    const int head = xcd * 4 + (j >> 3), qb = j & 7;
    const __amdgpu_buffer_rsrc_t ws = __builtin_amdgcn_make_buffer_rsrc(wsp, 0, WS_END, 0x00020000);
    const unsigned lds0 = (unsigned)(uintptr_t)lds;
    const int rowtile = qb * 8 + w;
    const bf16x8 q = ld16(ws, lane * 16, WS_Q + (unsigned)(head * 64 + rowtile) * 1024);
    {
        const char* src = wsp + WS_KV + (size_t)head * 131072 + w * 1024;
#pragma unroll
        for (int i = 0; i < 16; ++i) glds(lane * 16, src + i * 8192, lds0 + w * 1024 + i * 8192);
    }
    *(LAS u32x4*)(lds + LDS_ONES + threadIdx.x * 16) = (u32x4){0x3F803F80u, 0x3F803F80u, 0x3F803F80u, 0x3F803F80u};
    const bool vl = (lane & 16) == 0;
    const int idx = 0xDDDD;
    wait_bar<0>();
    f32x16 o, negm;
    {
        f32x16 s = MFMA(ldsr(lds + lane * 16), q, f32x16{});
        const float m = -halfmax(max16(s));
#pragma unroll
        for (int i = 0; i < 16; ++i) negm[i] = m;
    }
    for (int attempt = 0;; ++attempt) {
        o = f32x16{};
        ldsp kp = lds + lane * 16;
        ldsp vp = lds + (vl ? 1024 + (lane >> 5) * 256 : LDS_ONES) + (lane & 15) * 16;
        const unsigned vinc = vl ? 8192 : 0;
        f32x16 sa = MFMA(ldsr(kp), q, negm), sb;
        for (int it = 0; it < 16; ++it) {
            sb = MFMA(ldsr(kp + 2048), q, negm);
            exp16(sa); o = pv(ldsr(vp), sa, 0, o, idx); o = pv(ldsr(vp + 512), sa, 1, o, idx);
            sa = MFMA(ldsr(kp + 4096), q, negm);
            exp16(sb); o = pv(ldsr(vp + 2048), sb, 0, o, idx); o = pv(ldsr(vp + 2048 + 512), sb, 1, o, idx);
            sb = MFMA(ldsr(kp + 6144), q, negm);
            exp16(sa); o = pv(ldsr(vp + 4096), sa, 0, o, idx); o = pv(ldsr(vp + 4096 + 512), sa, 1, o, idx);
            sa = MFMA(ldsr(kp + 8192), q, negm);
            exp16(sb); o = pv(ldsr(vp + 6144), sb, 0, o, idx); o = pv(ldsr(vp + 6144 + 512), sb, 1, o, idx);
            kp += 8192; vp += vinc;
        }
        float chk = o[8];
#pragma unroll
        for (int i = 0; i < 8; ++i) chk = fmaxf(chk, fabsf(o[i]) * 0x1p-40f);
        bool ok = chk < 0x1p60f;
#pragma unroll
        for (int i = 0; i < 9; ++i) ok = ok && (o[i] == o[i]);
        if (attempt || __all(ok)) break;
        float m = -INFINITY;
        for (int t = 0; t < 64; ++t) m = fmaxf(m, max16(MFMA(ldsr(lds + t * 2048 + lane * 16), q, f32x16{})));
        m = -halfmax(m);
#pragma unroll
        for (int i = 0; i < 16; ++i) negm[i] = m;
    }
    st16(ws, lane * 16, WS_A + (unsigned)(rowtile * NKS + head) * 1024, pk8(o, 0, __builtin_amdgcn_rcpf(o[8])));
}

__global__ void __launch_bounds__(512, 2) k_final(char* wsp, char* out) {
    extern __shared__ __attribute__((aligned(16))) char smem[];
    ldsp lds = (ldsp)smem;
    const int lane = threadIdx.x & 63, w = __builtin_amdgcn_readfirstlane(threadIdx.x >> 6);
    const int xcd = blockIdx.x & 7, j = blockIdx.x >> 3;
    const int rp = xcd * 4 + (j >> 3), cp = j & 7;
    const __amdgpu_buffer_rsrc_t ws = __builtin_amdgcn_make_buffer_rsrc(wsp, 0, WS_END, 0x00020000);
    const unsigned sa = WS_A + (unsigned)(rp * 2 * NKS) * 1024, sw = WS_W + (unsigned)(cp * 2 * NKS) * 1024;
    const float g0 = __builtin_bit_cast(float, __builtin_amdgcn_raw_buffer_load_b32(ws, (lane & 31) * 4, WS_G + rp * 256, 0));
    const float g1 = __builtin_bit_cast(float, __builtin_amdgcn_raw_buffer_load_b32(ws, (lane & 31) * 4, WS_G + rp * 256 + 128, 0));
    f32x16 c[2][2] = {};
#pragma unroll 7
    for (int i = 0; i < 14; ++i) {
        const unsigned ko = (32 + w * 14 + i) * 1024;
        bf16x8 a0 = ld16(ws, lane * 16, sa + ko), a1 = ld16(ws, lane * 16, sa + NKS * 1024 + ko);
        bf16x8 w0 = ld16(ws, lane * 16, sw + ko), w1 = ld16(ws, lane * 16, sw + NKS * 1024 + ko);
        c[0][0] = MFMA(w0, a0, c[0][0]); c[0][1] = MFMA(w1, a0, c[0][1]); c[1][0] = MFMA(w0, a1, c[1][0]); c[1][1] = MFMA(w1, a1, c[1][1]);
    }
    f32x16 d[2][2] = {};
#pragma unroll
    for (int i = 0; i < 4; ++i) {
        const unsigned ko = (w * 4 + i) * 1024;
        bf16x8 a0 = ld16(ws, lane * 16, sa + ko), a1 = ld16(ws, lane * 16, sa + NKS * 1024 + ko);
        bf16x8 w0 = ld16(ws, lane * 16, sw + ko), w1 = ld16(ws, lane * 16, sw + NKS * 1024 + ko);
        d[0][0] = MFMA(w0, a0, d[0][0]); d[0][1] = MFMA(w1, a0, d[0][1]); d[1][0] = MFMA(w0, a1, d[1][0]); d[1][1] = MFMA(w1, a1, d[1][1]);
    }
#pragma unroll
    for (int a = 0; a < 2; ++a)
#pragma unroll
        for (int b = 0; b < 2; ++b) { const float g = a ? g1 : g0; f32x16 v = d[a][b] * g + c[a][b] * (1.f - g);
#pragma unroll
            for (int qd = 0; qd < 4; ++qd) *(LAS f32x4*)(lds + w * 16384 + ((a * 2 + b) * 4 + qd) * 1024 + lane * 16) = (f32x4){v[4 * qd], v[4 * qd + 1], v[4 * qd + 2], v[4 * qd + 3]}; }
    __syncthreads();
    f32x4 s0 = {}, s1 = {};
#pragma unroll
    for (int u = 0; u < 8; ++u) { s0 += *(LAS f32x4*)(lds + u * 16384 + (2 * w) * 1024 + lane * 16); s1 += *(LAS f32x4*)(lds + u * 16384 + (2 * w + 1) * 1024 + lane * 16); }
    const int row = rp * 64 + (w >> 2) * 32 + (lane & 31), col = cp * 64 + ((w >> 1) & 1) * 32 + (lane >> 5) * 16 + (w & 1) * 8;
    *(GAS u32x4*)(out + (size_t)row * 1024 + col * 2) = (u32x4){pk(s0[0], s0[1]), pk(s0[2], s0[3]), pk(s1[0], s1[1]), pk(s1[2], s1[3])};
}

extern "C" void kernel_launch(void* const* d_in, const int* in_sizes, int n_in, void* d_out, int out_size, void* d_ws, size_t ws_size, hipStream_t stream) {
    const char* x = (const char*)d_in[0]; const char* wqkv = (const char*)d_in[1]; const char* wo = (const char*)d_in[2];
    const char* w1 = (const char*)d_in[4]; const char* w2 = (const char*)d_in[5]; const char* gw = (const char*)d_in[6];
    (void)hipFuncSetAttribute((const void*)k_phase1, hipFuncAttributeMaxDynamicSharedMemorySize, 163840);
    (void)hipFuncSetAttribute((const void*)k_attn, hipFuncAttributeMaxDynamicSharedMemorySize, 163840);
    (void)hipFuncSetAttribute((const void*)k_final, hipFuncAttributeMaxDynamicSharedMemorySize, 163840);
    k_phase1<<<256, 512, 163840, stream>>>(x, wqkv, wo, w1, w2, gw, (char*)d_ws);
    k_attn<<<256, 512, 163840, stream>>>((char*)d_ws);
    k_final<<<256, 512, 131072, stream>>>((char*)d_ws, (char*)d_out);
}
```

```cpp
#include <hip/hip_runtime.h>
#include <stdint.h>

typedef __bf16 bf16x8 __attribute__((ext_vector_type(8)));
typedef __bf16 bf16x16 __attribute__((ext_vector_type(16)));
typedef __bf16 bf16x2 __attribute__((ext_vector_type(2)));
typedef short s16x4 __attribute__((ext_vector_type(4)));
typedef float f32x2 __attribute__((ext_vector_type(2)));
typedef float f32x4 __attribute__((ext_vector_type(4)));
typedef float f32x8 __attribute__((ext_vector_type(8)));
typedef float f32x16 __attribute__((ext_vector_type(16)));
typedef unsigned u32x2 __attribute__((ext_vector_type(2)));
typedef unsigned u32x4 __attribute__((ext_vector_type(4)));
#define LAS __attribute__((address_space(3)))
#define GAS __attribute__((address_space(1)))
#define DI __device__ __forceinline__
typedef LAS char* ldsp;
typedef __amdgpu_buffer_rsrc_t rsrc_t;

constexpr int NKS = 144;
constexpr unsigned WS_Q = 0;
constexpr unsigned WS_KV = WS_Q + 32u * 64 * 1024;
constexpr unsigned WS_A = WS_KV + 32u * 64 * 2048;
constexpr unsigned WS_W = WS_A + 32u * NKS * 2048;
constexpr unsigned WS_G = WS_W + 8u * NKS * 2048;
constexpr unsigned FQS = 32, FHS = 64;
constexpr unsigned WS_FQ = WS_G + 2048 * 4;
constexpr unsigned WS_FH = WS_FQ + 48 * 64 * FQS;
constexpr unsigned WS_E = WS_FH + 256 * FHS;
constexpr unsigned WS_END = WS_E + 256;
constexpr unsigned LDS_MRG = 131072 + 4096, LDS_Q = 147456, LDS_CNT = 163840 - 16;
constexpr int KV0 = 3, KV1 = 10;
#define FIDX(t) (((t) & 1) * 32 + ((t) >> 1))
constexpr float LOG2E = 1.4426950408889634f;
constexpr float QSCALE = 0.25f * LOG2E;
constexpr int SC1 = 16;

#define MFMA(a, b, c) __builtin_amdgcn_mfma_f32_32x32x16_bf16((a), (b), (c), 0, 0, 0)
#define SBAR() __builtin_amdgcn_sched_barrier(0)

DI void glds(unsigned voff, const void* sbase, unsigned lds) {
    asm volatile("s_mov_b32 m0, %2\n\ts_nop 0\n\tglobal_load_lds_dwordx4 %0, %1" :: "v"(voff), "s"(sbase), "s"(lds) : "memory"); }
DI void glds_sc1(unsigned voff, const void* sbase, unsigned lds) {
    asm volatile("s_mov_b32 m0, %2\n\ts_nop 0\n\tglobal_load_lds_dwordx4 %0, %1 sc1" :: "v"(voff), "s"(sbase), "s"(lds) : "memory"); }
template <int N> DI void wait_bar() { asm volatile("s_waitcnt vmcnt(%0) lgkmcnt(0)\n\ts_barrier" :: "i"(N) : "memory"); }
DI void lds_bar() { asm volatile("s_waitcnt lgkmcnt(0)\n\ts_barrier" ::: "memory"); }
DI unsigned pk(float a, float b) { f32x2 v = {a, b}; return __builtin_bit_cast(unsigned, __builtin_convertvector(v, bf16x2)); }
DI u32x4 pk8(const f32x16& a, int o) { return (u32x4){pk(a[o], a[o + 1]), pk(a[o + 2], a[o + 3]), pk(a[o + 4], a[o + 5]), pk(a[o + 6], a[o + 7])}; }
DI bf16x8 ldsr(ldsp p) { return *(const LAS bf16x8*)p; }
DI bf16x8 ldtr(ldsp p, int d = 512) {
    s16x4 a = __builtin_amdgcn_ds_read_tr16_b64_v4i16((LAS s16x4*)p), b = __builtin_amdgcn_ds_read_tr16_b64_v4i16((LAS s16x4*)(p + d));
    return __builtin_bit_cast(bf16x8, __builtin_shufflevector(a, b, 0, 1, 2, 3, 4, 5, 6, 7)); }
DI void st16(rsrc_t r, unsigned off, u32x4 v) { __builtin_amdgcn_raw_buffer_store_b128(v, r, off, 0, SC1); }
DI u32x4 ld16u(rsrc_t r, unsigned voff, unsigned soff) { return __builtin_amdgcn_raw_buffer_load_b128(r, voff, soff, SC1); }
DI bf16x8 ld16(rsrc_t r, unsigned voff, unsigned soff) { return __builtin_bit_cast(bf16x8, ld16u(r, voff, soff)); }
DI float sigmoidf(float v) { return __builtin_amdgcn_rcpf(1.f + __builtin_amdgcn_exp2f(-LOG2E * v)); }
DI float halfmax(float m) { auto rr = __builtin_amdgcn_permlane32_swap(__float_as_uint(m), __float_as_uint(m), false, false); return fmaxf(__uint_as_float(rr[0]), __uint_as_float(rr[1])); }
DI float max16(const f32x16& s) { float m = fmaxf(fmaxf(s[0], s[1]), s[2]);
#pragma unroll
    for (int i = 3; i < 15; i += 2) m = fmaxf(fmaxf(m, s[i]), s[i + 1]);
    return fmaxf(m, s[15]); }
DI f32x16 splat16(float v) { f32x16 r;
#pragma unroll
    for (int i = 0; i < 16; ++i) r[i] = v;
    return r; }


struct P1 { const char* xs; const char* ws; const char* u0; unsigned vx0, vx1, vw, vu, dst; int rot, rot2; };
constexpr int XS(int k) { return (k == 8 ? 1 : k > 8 ? k - 7 : (k & 3) == 3 ? 0 : (k & 3) + 2) * 16384; }
constexpr int WS(int k) { return 81920 + (k == 8 ? 0 : k > 8 ? k - 8 : (k & 3) + 1) * 16384; }
template <int K, int I, int NT> DI void issue1(const P1& p) {
    if constexpr (K >= 0 && K < 8) {
        if constexpr (I < 2) glds(I ? p.vx1 : p.vx0, p.xs + ((K + p.rot) & 7) * 128, p.dst + XS(K) + I * 1024);
        else if constexpr (NT == 2 || I == 2) glds(p.vw, p.ws + (size_t)((((K + p.rot) & 7) * 64 + 8 * (I - 2)) * 3072), p.dst + WS(K) + (I - 2) * 1024);
    } else if constexpr (K >= 8 && I < 2) glds(p.vu, p.u0 + (size_t)((((2 * K + p.rot2) & 7) * 64 + 8 * I) * 3584), p.dst + XS(K) + I * 1024);
    else if constexpr (K >= 8) glds(p.vu, p.u0 + 128 + (size_t)((((2 * K + p.rot2) & 7) * 64 + 8 * (I - 2)) * 3584), p.dst + WS(K) + (I - 2) * 1024);
}
template <int K> DI void issue(const P1& p, bool two) { issue1<K, 0, 2>(p); issue1<K, 1, 2>(p); issue1<K, 2, 2>(p); if (two) issue1<K, 3, 2>(p); }
constexpr int NPRO = 3;
constexpr int ord(int i) { constexpr int t[11] = {0, 1, 2, 3, 8, 4, 5, 6, 7, 9, 10}; return t[i]; }
constexpr int pos(int k) { int i = 0; while (ord(i) != k) ++i; return i; }
constexpr int wait1(int k, int nt) { int n = pos(k) < NPRO ? 1 : 0; for (int i = pos(k) + 1; i < NPRO + k; ++i) n += ord(i) < 8 ? 2 + nt : 4; return n; }
struct WF { bf16x8 f[2][2]; };
template <bool SWAP> DI f32x16 mm(const bf16x8& wf, const bf16x8& xf, const f32x16& c) { if constexpr (SWAP) return MFMA(xf, wf, c); else return MFMA(wf, xf, c); }
template <int N> DI void vm_bar() { asm volatile("s_waitcnt vmcnt(%0)\n\ts_barrier" :: "i"(N) : "memory"); }

template <int KK, bool SWAP, int NT> DI void pass1_step(const P1& p, ldsp lds, const unsigned (&xa)[4], unsigned wa0, unsigned wa1, bf16x8 (&xf)[32], WF& wb, f32x16& c0, f32x16& c1) {
    if constexpr (KK == 7) wait_bar<wait1(KK, NT)>(); else vm_bar<wait1(KK, NT)>();
    constexpr int K = ord(NPRO + KK);
    ldsp sx = lds + XS(KK), s0 = lds + WS(KK) + wa0, s1 = lds + WS(KK) + wa1;
    WF wa;
    if constexpr (KK > 0) c0 = mm<SWAP>(wb.f[0][0], xf[KK * 4 - 2], c0);
    xf[KK * 4] = ldsr(sx + xa[0]); wa.f[0][0] = ldtr(s0, NT * 256); SBAR();
    if constexpr (NT == 2) { if constexpr (KK > 0) c1 = mm<SWAP>(wb.f[0][1], xf[KK * 4 - 2], c1); wa.f[0][1] = ldtr(s1); SBAR(); }
    issue1<K, 0, NT>(p); SBAR();
    if constexpr (KK > 0) c0 = mm<SWAP>(wb.f[1][0], xf[KK * 4 - 1], c0);
    xf[KK * 4 + 1] = ldsr(sx + xa[1]); wa.f[1][0] = ldtr(s0 + 2048, NT * 256); SBAR();
    if constexpr (NT == 2) { if constexpr (KK > 0) c1 = mm<SWAP>(wb.f[1][1], xf[KK * 4 - 1], c1); wa.f[1][1] = ldtr(s1 + 2048); SBAR(); }
    issue1<K, 1, NT>(p); SBAR();
    c0 = mm<SWAP>(wa.f[0][0], xf[KK * 4], c0);
    xf[KK * 4 + 2] = ldsr(sx + xa[2]); wb.f[0][0] = ldtr(s0 + 4096, NT * 256); SBAR();
    if constexpr (NT == 2) { c1 = mm<SWAP>(wa.f[0][1], xf[KK * 4], c1); wb.f[0][1] = ldtr(s1 + 4096); SBAR(); }
    issue1<K, 2, NT>(p); SBAR();
    c0 = mm<SWAP>(wa.f[1][0], xf[KK * 4 + 1], c0);
    xf[KK * 4 + 3] = ldsr(sx + xa[3]); wb.f[1][0] = ldtr(s0 + 6144, NT * 256); SBAR();
    if constexpr (NT == 2) { c1 = mm<SWAP>(wa.f[1][1], xf[KK * 4 + 1], c1); wb.f[1][1] = ldtr(s1 + 6144); SBAR(); }
    issue1<K, 3, NT>(p); SBAR();
    if constexpr (KK == 7) { c0 = mm<SWAP>(wb.f[0][0], xf[30], c0); if constexpr (NT == 2) c1 = mm<SWAP>(wb.f[0][1], xf[30], c1); c0 = mm<SWAP>(wb.f[1][0], xf[31], c0); if constexpr (NT == 2) c1 = mm<SWAP>(wb.f[1][1], xf[31], c1); }
}
template <bool SWAP, int NT> DI void pass1(const P1& p, ldsp lds, const unsigned (&xa)[4], unsigned wa0, unsigned wa1, bf16x8 (&xf)[32], f32x16& c0, f32x16& c1) {
    WF wb;
    pass1_step<0, SWAP, NT>(p, lds, xa, wa0, wa1, xf, wb, c0, c1); pass1_step<1, SWAP, NT>(p, lds, xa, wa0, wa1, xf, wb, c0, c1);
    pass1_step<2, SWAP, NT>(p, lds, xa, wa0, wa1, xf, wb, c0, c1); pass1_step<3, SWAP, NT>(p, lds, xa, wa0, wa1, xf, wb, c0, c1);
    pass1_step<4, SWAP, NT>(p, lds, xa, wa0, wa1, xf, wb, c0, c1); pass1_step<5, SWAP, NT>(p, lds, xa, wa0, wa1, xf, wb, c0, c1);
    pass1_step<6, SWAP, NT>(p, lds, xa, wa0, wa1, xf, wb, c0, c1); pass1_step<7, SWAP, NT>(p, lds, xa, wa0, wa1, xf, wb, c0, c1);
}
template <int KK> DI void pass2_step(ldsp lds, unsigned wa, const bf16x8 (&xf)[32], WF& wb, f32x16& c0, f32x16& c1) {
    ldsp sb = lds + XS(8 + KK / 2) + (KK & 1) * 8192, s0 = sb + wa, s1 = sb + (wa ^ 64);
    WF wa_;
    if constexpr (KK > 0) c0 = MFMA(wb.f[0][0], xf[KK * 4 - 2], c0);
    wa_.f[0][0] = ldtr(s0); SBAR();
    if constexpr (KK > 0) c1 = MFMA(wb.f[0][1], xf[KK * 4 - 2], c1);
    wa_.f[0][1] = ldtr(s1); SBAR();
    if constexpr (KK > 0) c0 = MFMA(wb.f[1][0], xf[KK * 4 - 1], c0);
    wa_.f[1][0] = ldtr(s0 + 2048); SBAR();
    if constexpr (KK > 0) c1 = MFMA(wb.f[1][1], xf[KK * 4 - 1], c1);
    wa_.f[1][1] = ldtr(s1 + 2048); SBAR();
    c0 = MFMA(wa_.f[0][0], xf[KK * 4], c0); wb.f[0][0] = ldtr(s0 + 4096); SBAR();
    c1 = MFMA(wa_.f[0][1], xf[KK * 4], c1); wb.f[0][1] = ldtr(s1 + 4096); SBAR();
    c0 = MFMA(wa_.f[1][0], xf[KK * 4 + 1], c0); wb.f[1][0] = ldtr(s0 + 6144); SBAR();
    c1 = MFMA(wa_.f[1][1], xf[KK * 4 + 1], c1); wb.f[1][1] = ldtr(s1 + 6144); SBAR();
    if constexpr (KK == 7) { c0 = MFMA(wb.f[0][0], xf[30], c0); c1 = MFMA(wb.f[0][1], xf[30], c1); c0 = MFMA(wb.f[1][0], xf[31], c0); c1 = MFMA(wb.f[1][1], xf[31], c1); }
}
static_assert(WS(9) == WS(8) + 16384 && WS(10) == WS(8) + 32768 && WS(11) == WS(8) + 49152, "");
template <int I> DI ldsp w1_frag(ldsp b0, ldsp b1) { return (I < 32 ? b0 : b1) + (I & 31) * 2048; }
constexpr int silu_seq(int n) {
    int i = 0;
    for (int s = 0; s < 22; ++s) { for (int st = 0; st < 5; ++st) { const int e = s - st; if (e >= 0 && e < 16 && i++ == n) return st * 16 + e; }
        const int e = s - 5; if (e >= 0 && e < 16 && (e & 1) && i++ == n) return 80 + e / 2; }
    return -1; }
template <int N> DI void silu_op(const f32x16& c, float (&t)[16], u32x4 (&hv)[2]) {
    if constexpr (N >= 0 && N < 16) asm volatile("v_mul_f32 %0, 0xbfb8aa3b, %1" : "=v"(t[N]) : "v"(c[N]));
    else if constexpr (N >= 16 && N < 32) asm volatile("v_exp_f32 %0, %0" : "+v"(t[N - 16]));
    else if constexpr (N >= 32 && N < 48) asm volatile("v_add_f32 %0, 1.0, %0" : "+v"(t[N - 32]));
    else if constexpr (N >= 48 && N < 64) asm volatile("v_rcp_f32 %0, %0" : "+v"(t[N - 48]));
    else if constexpr (N >= 64 && N < 80) asm volatile("v_mul_f32 %0, %0, %1" : "+v"(t[N - 64]) : "v"(c[N - 64]));
    else if constexpr (N >= 80 && N < 88) { unsigned r; asm volatile("v_cvt_pk_bf16_f32 %0, %1, %2" : "=v"(r) : "v"(t[2 * N - 160]), "v"(t[2 * N - 159])); hv[(N - 80) / 4][(N - 80) & 3] = r; }
}
template <int I, class F> DI void chain_step(ldsp lds, ldsp wa, const bf16x8 (&xf)[32], bf16x8 (&wf)[4], f32x16& c0, f32x16& c1, float (&t)[16], u32x4 (&hv)[2], F& mid) {
    if constexpr (I < 32) c0 = MFMA(wf[I & 3], xf[I], c0); else c1 = MFMA(wf[I & 3], xf[I - 32], c1);
    if constexpr (I + 4 < 64) wf[I & 3] = ldtr(w1_frag<I + 4>(lds, wa));
    SBAR();
    if constexpr (I == 19) { vm_bar<3>(); SBAR(); }
    if constexpr (I == 30) { mid(); SBAR(); }
    if constexpr (I >= 36) { constexpr int n = (I - 36) * 22 / 7; silu_op<silu_seq(n)>(c0, t, hv); silu_op<silu_seq(n + 1)>(c0, t, hv); silu_op<silu_seq(n + 2)>(c0, t, hv); if constexpr ((I - 35) * 22 / 7 > n + 3) silu_op<silu_seq(n + 3)>(c0, t, hv); SBAR(); }
    if constexpr (I < 63) chain_step<I + 1>(lds, wa, xf, wf, c0, c1, t, hv, mid);
}
DI unsigned h_off(int rowtile, int ftile, int lane) { return WS_A + (unsigned)((rowtile >> 1) * NKS + 32 + 2 * ftile) * 2048 + (rowtile & 1) * 1024 + lane * 16; }
DI f32x16 pv(bf16x8 v, const f32x16& p, int o, f32x16 acc) {
    f32x8 b = o ? __builtin_shufflevector(p, p, 8, 9, 10, 11, 12, 13, 14, 15) : __builtin_shufflevector(p, p, 0, 1, 2, 3, 4, 5, 6, 7);
    return __builtin_amdgcn_smfmac_f32_32x32x32_bf16(v, __builtin_bit_cast(bf16x16, b), acc, 0xDDDD, 0, 0); }

__global__ void __launch_bounds__(512, 2) k_fused(const char* x, const char* wqkv, const char* wo, const char* w1, const char* w2, const char* gw, char* wsp, char* out) {
    extern __shared__ __attribute__((aligned(16))) char smem[];
    ldsp lds = (ldsp)smem;
    const unsigned lds0 = (unsigned)(uintptr_t)lds;
    const int lane = threadIdx.x & 63, w = __builtin_amdgcn_readfirstlane(threadIdx.x >> 6);
    const int xcd = blockIdx.x & 7, j = blockIdx.x >> 3;
    const char* pc0; unsigned clen;
    asm volatile("s_getpc_b64 %0\nLgm_code_begin:\n\ts_mov_b32 %1, Lgm_code_end-Lgm_code_begin-128" : "=s"(pc0), "=s"(clen));
    const rsrc_t ws = __builtin_amdgcn_make_buffer_rsrc(wsp, 0, WS_END, 0x00020000);
    const int r = lane & 31, h = lane >> 5;
    const int head = xcd * 4 + (j >> 3), qrt = (j & 7) * 8 + (w & 3) * 2;
    const unsigned oslot_u = WS_A + (unsigned)((qrt >> 1) * NKS + head) * 2048 + (w >> 2) * 1024;
    const int kh = w >> 2;
    const int rp = xcd * 4 + (j >> 3), cp = j & 7;
    u32x2 tag;
    f32x16 o0, o1, c00, c01, c10, c11, vv; unsigned goff, ooff;
    {
        const int rb = (xcd >> 1) * 4 + (j >> 3), cb = (xcd & 1) * 8 + (j & 7);
        const int rt = w & 3, ch = w >> 2;
        P1 p;
        p.rot = ((xcd >> 1) + ((rb + cb) & 1) * 4 + (xcd & 1) * 2) & 7;
        p.dst = lds0 + w * 2048;
        const int un1 = ((3 * cb) >> 1) + ch;
        p.vx0 = (rb * 128 + 16 * w + (lane >> 3)) * 1024 + (((lane & 7) ^ (lane >> 4)) << 4);
        p.vx1 = (rb * 128 + 16 * w + (lane >> 3) + 8) * 1024 + (((lane & 7) ^ (4 + (lane >> 4))) << 4);
        const unsigned wsw = ((lane & 7) ^ (((lane >> 4) & 1) << 2)) << 4;
        const bool two = ch == (cb & 1);
        unsigned vw2 = (lane >> 3) * 3072 + wsw, vw1 = (lane >> 2) * 3072 + (lane & 3) * 16 + (cb & 1) * 64;
        asm volatile("" : "+v"(vw1), "+v"(vw2));
        p.vw = un1 * 128 + 16 * rt * 3072 + (two ? vw2 : vw1);
        asm volatile("" : "+v"(p.vx0), "+v"(p.vx1), "+v"(p.vw));
        SBAR();
        p.xs = x; p.ws = wqkv;
        const bool full2 = (cb & 3) != 3;
        issue<0>(p, two);
        u32x2 ep; unsigned pf0, pf1, pf2;
        asm volatile("buffer_load_dwordx2 %0, off, %1, %2 sc1" : "={v[254:255]}"(ep) : "s"(ws), "s"(WS_E) : "memory");
        {
            const char* pc = (const char*)((unsigned long long)pc0 & ~63ull) + min((unsigned)(j * 8 + w) * 192u, clen);
            asm volatile("s_load_dword %0, %3, 0x0\n\ts_load_dword %1, %3, 0x40\n\ts_load_dword %2, %3, 0x80" : "=&s"(pf0), "=&s"(pf1), "=&s"(pf2) : "s"(pc) : "memory");
        }
        issue<1>(p, two); issue<2>(p, two);
        st16(ws, oslot_u + lane * 16, (u32x4){~0u, ~0u, ~0u, ~0u});
        const int un2 = (cb >> 2) * 7 + (cb & 3) * 2 - (full2 ? 0 : 1);
        const char* u0 = w1 + un2 * 128 + (size_t)(16 * rt) * 3584;
        const unsigned vu = (lane >> 3) * 3584 + wsw;
        p.u0 = u0; p.rot2 = p.rot + ch; p.vu = vu;
        unsigned xa[4];
#pragma unroll
        for (int s = 0; s < 4; ++s) xa[s] = (rt * 32 + r) * 128 + (((2 * s + h) ^ ((r >> 1) & 7)) << 4);
        const int q4 = (lane & 15) >> 2, p4 = lane & 3, blk = (lane >> 4) & 1;
        const unsigned wa = (8 * h + q4) * 128 + ((blk * 32 + p4 * 8) ^ ((q4 >> 1) << 6));
        bf16x8 xf[32]; f32x16 c0 = {}, c1 = {};
        const int rowtile = rb * 4 + rt;
        unsigned fq; bool nst2;
        {
            const int ct = two ? 2 * un1 : 2 * un1 + (cb & 1), ty = ct >> 4;
            const unsigned wa0 = ch * 8192 + (two ? wa : (8 * h + q4) * 64 + blk * 32 + p4 * 8), wa1 = wa0 ^ 64;
            fq = WS_FQ + (unsigned)(ct * 64 + FIDX(rowtile)) * FQS + (two ? (lane & 1) * 64 * FQS : 0);
            asm volatile("s_waitcnt lgkmcnt(0)" :: "s"(pf0), "s"(pf1), "s"(pf2), "v"(wa0), "v"(wa1), "v"(xa[0]), "v"(xa[1]), "v"(xa[2]), "v"(xa[3]), "v"(vu) : "memory");
            if (ty == 2) { if (two) pass1<true, 2>(p, lds, xa, wa0, wa1, xf, c0, c1); else pass1<true, 1>(p, lds, xa, wa0, wa1, xf, c0, c1); }
            else { if (two) pass1<false, 2>(p, lds, xa, wa0, wa1, xf, c0, c1); else pass1<false, 1>(p, lds, xa, wa0, wa1, xf, c0, c1); }
            unsigned e0, e1;
            asm volatile("v_readfirstlane_b32 %0, v254\n\tv_readfirstlane_b32 %1, v255" : "=&s"(e0), "=&s"(e1) : "{v[254:255]}"(ep));
            const unsigned long long e = (((unsigned long long)e1 << 32) | e0) + 1;
            tag = (u32x2){(unsigned)e, (unsigned)(e >> 32)};
            const unsigned hr = (unsigned)((ct & 15) * 128 + rowtile);
            if (ty == 0) {
#pragma unroll
                for (int i = 0; i < 16; ++i) { float t0 = c0[i] * QSCALE, t1 = c1[i] * QSCALE; asm volatile("" : "+v"(t0)); asm volatile("" : "+v"(t1)); c0[i] = t0; c1[i] = t1; }
            }
            const unsigned o = ty == 0 ? WS_Q + hr * 1024 + lane * 16
                             : ty == 1 ? WS_KV + hr * 2048 + ((lane & 0x33) | ((lane & 4) << 1) | ((lane & 8) >> 1)) * 16
                                       : WS_KV + hr * 2048 + 1024 + blk * 131072 + h * 512 + (lane & 15) * 16;
            const unsigned d1 = ty == 0 ? 65536 : ty == 1 ? 131072 : 256, d2 = ty == 0 ? 131072 : 262144;
            st16(ws, o, pk8(c0, 0)); st16(ws, o + d1, pk8(c0, 8));
            if (two) { st16(ws, o + d2, pk8(c1, 0)); st16(ws, o + d2 + d1, pk8(c1, 8)); }
            nst2 = two;
        }
        c0 = f32x16{}; c1 = f32x16{};
        const unsigned ho = h_off(rowtile, 2 * (un2 + ch), lane);
#define PUBLISH(N) do { asm volatile("s_waitcnt vmcnt(%0)" :: "i"(N) : "memory"); if (lane < 2) __builtin_amdgcn_raw_buffer_store_b64(tag, ws, fq, 0, SC1); } while (0)
#define BAR0B() do { if (nst2) vm_bar<4>(); else vm_bar<2>(); } while (0)
        const unsigned fo = WS_FQ + ((unsigned)(head >> 1) * 64 + (lane < 32 ? (16 + 16 * (rt & 1)) * 64 + FIDX(2 * lane + (rt >> 1)) : FIDX(qrt + (lane & 1)))) * FQS;
        bool rdy = false;
        const int bl = ch ? blockIdx.x * 4 + rt : 1024 + (rb * 4 + (cb >> 2)) * 2 + (rt & 1);
        const unsigned bs = (ch ? LDS_Q + 8192 : XS(10)) + rt * 2048;
        auto blk_load = [&]() { const char* s = (bl < 256 ? wo + (size_t)(bl >> 3) * 16384 : w2 + (size_t)((bl >> 3) - 32) * 16384) + (bl & 7) * 128;
            glds((lane >> 3) * 1024 + wsw, s, lds0 + bs); glds((lane >> 3) * 1024 + wsw, s + 8192, lds0 + bs + 1024); };
        auto blk_store = [&]() {
            asm volatile("s_waitcnt vmcnt(0)" ::: "memory");
            const unsigned ta = bs + (4 * h + q4) * 128 + ((blk * 16 + (p4 & 1) * 32 + (p4 >> 1) * 8) ^ ((q4 >> 1) << 6));
#pragma unroll
            for (int i = 0; i < 2; ++i) { ldsp tp = lds + (ta ^ (i * 64));
                s16x4 a = __builtin_amdgcn_ds_read_tr16_b64_v4i16((LAS s16x4*)tp), b = __builtin_amdgcn_ds_read_tr16_b64_v4i16((LAS s16x4*)(tp + 1024));
                st16(ws, WS_W + (unsigned)((bl & 7) * NKS + (bl >> 3)) * 2048 + i * 1024 + lane * 16, __builtin_bit_cast(u32x4, __builtin_shufflevector(a, b, 0, 1, 2, 3, 4, 5, 6, 7))); }
            asm volatile("" ::: "memory");
            if (lane == 0 && w == 7) *(LAS u32x4*)(lds + LDS_CNT) = (u32x4){0u, 0u, 0u, 0u};
        };
        if (ch) {
            BAR0B();
#pragma unroll
            for (int c = 6; c < 8; ++c) { const char* s = u0 + (full2 ? 0 : 128) + (size_t)(((c + p.rot) & 7) * 64 * 3584); const unsigned d = lds0 + (full2 ? XS(11) : WS(11)) + (c & 1) * 8192 + rt * 2048; glds(vu, s, d); glds(vu, s + 8 * 3584, d + 1024); }
#pragma unroll
            for (int c = 6; c < 8; ++c) { const char* s = u0 + 128 + (size_t)(((c + p.rot) & 7) * 64 * 3584); const unsigned d = lds0 + WS(11) + (c & 1) * 8192 + rt * 2048; glds(vu, s, d); glds(vu, s + 8 * 3584, d + 1024); }
            blk_load();
            PUBLISH(10);
            ldsp b0 = lds + WS(8) + wa, b1 = lds + WS(8) + (wa ^ 64);
            asm volatile("" : "+v"(b0), "+v"(b1));
            bf16x8 wf[4] = {ldtr(b0), ldtr(b0 + 2048), ldtr(b0 + 4096), ldtr(b0 + 6144)};
            float t[16]; u32x4 h0[2];
            chain_step<0>(b0, b1, xf, wf, c0, c1, t, h0, blk_store);
            lds_bar();
            __builtin_amdgcn_s_setprio(3);
            st16(ws, ho, h0[0]); st16(ws, ho + 2048, h0[1]);
        } else if (full2) {
            WF wb;
            pass2_step<0>(lds, wa, xf, wb, c0, c1); pass2_step<1>(lds, wa, xf, wb, c0, c1);
            BAR0B();
            __builtin_amdgcn_s_setprio(1);
            pass2_step<2>(lds, wa, xf, wb, c0, c1);
            PUBLISH(0);
            pass2_step<3>(lds, wa, xf, wb, c0, c1); pass2_step<4>(lds, wa, xf, wb, c0, c1); pass2_step<5>(lds, wa, xf, wb, c0, c1);
            asm volatile("s_barrier" ::: "memory");
            pass2_step<6>(lds, wa, xf, wb, c0, c1); pass2_step<7>(lds, wa, xf, wb, c0, c1);
            SBAR();
            __builtin_amdgcn_s_setprio(0);
            SBAR();
        } else {
            BAR0B();
            PUBLISH(0);
            if (cb == 3) glds(lane * 16, gw, lds0 + XS(11) + rt * 1024);
            blk_load();
            asm volatile("s_barrier" ::: "memory");
            if (cb == 3) {
                asm volatile("s_waitcnt vmcnt(2)" ::: "memory");
#pragma unroll
                for (int k = 0; k < 32; ++k) c0 = MFMA(ldsr(lds + XS(11) + rt * 1024 + (((k >> 2) + p.rot) & 7) * 128 + (k & 3) * 32 + h * 16), xf[k], c0);
                if (h == 0) __builtin_amdgcn_raw_buffer_store_b32(__builtin_bit_cast(unsigned, sigmoidf(c0[0])), ws, WS_G + rowtile * 128 + r * 4, 0, SC1);
            }
        }
        auto kv_issue = [&]() {
            const char* qs = wsp + WS_Q + (size_t)(head * 64 + qrt) * 1024;
            glds_sc1(lane * 16, qs, lds0 + LDS_Q + rt * 2048); glds_sc1(lane * 16, qs + 1024, lds0 + LDS_Q + rt * 2048 + 1024);
            const char* src = wsp + WS_KV + (size_t)head * 131072 + rt * 1024;
#pragma unroll 1
            for (int i = 0; i < KV1; ++i) glds_sc1(lane * 16, src + i * 4096, lds0 + rt * 1024 + i * 4096);
        };
        u32x4 hv[4];
        goff = lane * 16;
        ooff = (rp * 64 + kh * 32 + r) * 1024 + (cp * 64 + ((w >> 1) & 1) * 32 + h * 16 + (w & 1) * 8) * 2;
        const unsigned one = (r == 16 || r == 20) ? 0x3F803F80u : 0u;
        if (!ch) {
            if (full2) {
                f32x16 st0, st1; u32x2 fe;
                asm volatile("buffer_load_dwordx2 %0, %1, %2, 0 offen sc1" : "={v[224:225]}"(fe) : "v"(fo), "s"(ws) : "memory");
            asm volatile(
        "v_mov_b32 v120, 0\n\t"
        "v_mov_b32 v121, 0\n\t"
        "v_mov_b32 v122, 0\n\t"
        "v_mov_b32 v123, 0\n\t"
        "v_mov_b32 v124, %[one]\n\t"
        "v_mov_b32 v125, %[one]\n\t"
        "v_mov_b64 v[126:127], v[124:125]\n\t"
        "v_mov_b64 v[128:129], v[124:125]\n\t"
        "v_mov_b64 v[130:131], v[124:125]\n\t"
        "v_mov_b64 v[132:133], v[124:125]\n\t"
        "v_mov_b64 v[134:135], v[124:125]\n\t"
        "v_mov_b64 v[136:137], v[124:125]\n\t"
        "v_mov_b64 v[138:139], v[124:125]\n\t"
        "v_mul_f32 v80, %[nl], v16\n\t"
        "v_mul_f32 v81, %[nl], v17\n\t"
        "v_mul_f32 v82, %[nl], v18\n\t"
        "v_mul_f32 v83, %[nl], v19\n\t"
        "v_mul_f32 v84, %[nl], v20\n\t"
        "v_mul_f32 v85, %[nl], v21\n\t"
        "v_mul_f32 v86, %[nl], v22\n\t"
        "v_mul_f32 v87, %[nl], v23\n\t"
        "v_mul_f32 v88, %[nl], v24\n\t"
        "v_mul_f32 v89, %[nl], v25\n\t"
        "v_mul_f32 v90, %[nl], v26\n\t"
        "v_mul_f32 v91, %[nl], v27\n\t"
        "v_mul_f32 v92, %[nl], v28\n\t"
        "v_mul_f32 v93, %[nl], v29\n\t"
        "v_mul_f32 v94, %[nl], v30\n\t"
        "v_mul_f32 v95, %[nl], v31\n\t"
        "v_mul_f32 v96, %[nl], v32\n\t"
        "v_mul_f32 v97, %[nl], v33\n\t"
        "v_mul_f32 v98, %[nl], v34\n\t"
        "v_mul_f32 v99, %[nl], v35\n\t"
        "v_mul_f32 v100, %[nl], v36\n\t"
        "v_mul_f32 v101, %[nl], v37\n\t"
        "v_mul_f32 v102, %[nl], v38\n\t"
        "v_mul_f32 v103, %[nl], v39\n\t"
        "v_mul_f32 v104, %[nl], v40\n\t"
        "v_mul_f32 v105, %[nl], v41\n\t"
        "v_mul_f32 v106, %[nl], v42\n\t"
        "v_mul_f32 v107, %[nl], v43\n\t"
        "v_mul_f32 v108, %[nl], v44\n\t"
        "v_mul_f32 v109, %[nl], v45\n\t"
        "v_mul_f32 v110, %[nl], v46\n\t"
        "v_mul_f32 v111, %[nl], v47\n\t"
        "v_mfma_f32_32x32x16_bf16 v[48:63], v[120:123], v[120:123], 0\n\t"
        "v_exp_f32 v80, v80\n\t"
        "v_exp_f32 v81, v81\n\t"
        "v_exp_f32 v82, v82\n\t"
        "v_exp_f32 v83, v83\n\t"
        "v_mfma_f32_32x32x16_bf16 v[64:79], v[120:123], v[120:123], 0\n\t"
        "v_exp_f32 v84, v84\n\t"
        "v_exp_f32 v85, v85\n\t"
        "v_exp_f32 v86, v86\n\t"
        "v_exp_f32 v87, v87\n\t"
        "v_mfma_f32_32x32x16_bf16 v[144:159], v[120:123], v[120:123], 0\n\t"
        "v_exp_f32 v88, v88\n\t"
        "v_exp_f32 v89, v89\n\t"
        "v_exp_f32 v90, v90\n\t"
        "v_exp_f32 v91, v91\n\t"
        "v_mfma_f32_32x32x16_bf16 v[160:175], v[120:123], v[120:123], 0\n\t"
        "v_exp_f32 v92, v92\n\t"
        "v_exp_f32 v93, v93\n\t"
        "v_exp_f32 v94, v94\n\t"
        "v_exp_f32 v95, v95\n\t"
        "v_mfma_f32_32x32x16_bf16 v[176:191], v[120:123], v[120:123], 0\n\t"
        "v_exp_f32 v96, v96\n\t"
        "v_exp_f32 v97, v97\n\t"
        "v_exp_f32 v98, v98\n\t"
        "v_exp_f32 v99, v99\n\t"
        "v_mfma_f32_32x32x16_bf16 v[192:207], v[120:123], v[120:123], 0\n\t"
        "v_exp_f32 v100, v100\n\t"
        "v_exp_f32 v101, v101\n\t"
        "v_exp_f32 v102, v102\n\t"
        "v_exp_f32 v103, v103\n\t"
        "v_exp_f32 v104, v104\n\t"
        "v_exp_f32 v105, v105\n\t"
        "v_exp_f32 v106, v106\n\t"
        "v_exp_f32 v107, v107\n\t"
        "v_exp_f32 v108, v108\n\t"
        "v_exp_f32 v109, v109\n\t"
        "v_exp_f32 v110, v110\n\t"
        "v_exp_f32 v111, v111\n\t"
                : "={v[80:95]}"(st0), "={v[96:111]}"(st1), "={v[48:63]}"(o0), "={v[64:79]}"(o1), "=&{v[124:139]}"(vv), "={v[144:159]}"(c00), "={v[160:175]}"(c01), "={v[176:191]}"(c10), "={v[192:207]}"(c11), "+{v142}"(goff), "+v"(ooff)
                : "{v[16:31]}"(c0), "{v[32:47]}"(c1), [nl] "s"(0xbfb8aa3bu), [one] "v"(one)
                : "v120", "v121", "v122", "v123");

                asm volatile("s_waitcnt vmcnt(0)" : "+{v[224:225]}"(fe) :: "memory");
                rdy = __all(fe[0] == tag[0] && fe[1] == tag[1]);
                if (rdy) { kv_issue(); }
            asm volatile(
        "v_add_f32 v80, 1.0, v80\n\t"
        "v_add_f32 v81, 1.0, v81\n\t"
        "v_add_f32 v82, 1.0, v82\n\t"
        "v_add_f32 v83, 1.0, v83\n\t"
        "v_add_f32 v84, 1.0, v84\n\t"
        "v_add_f32 v85, 1.0, v85\n\t"
        "v_add_f32 v86, 1.0, v86\n\t"
        "v_add_f32 v87, 1.0, v87\n\t"
        "v_add_f32 v88, 1.0, v88\n\t"
        "v_add_f32 v89, 1.0, v89\n\t"
        "v_add_f32 v90, 1.0, v90\n\t"
        "v_add_f32 v91, 1.0, v91\n\t"
        "v_add_f32 v92, 1.0, v92\n\t"
        "v_add_f32 v93, 1.0, v93\n\t"
        "v_add_f32 v94, 1.0, v94\n\t"
        "v_add_f32 v95, 1.0, v95\n\t"
        "v_add_f32 v96, 1.0, v96\n\t"
        "v_add_f32 v97, 1.0, v97\n\t"
        "v_add_f32 v98, 1.0, v98\n\t"
        "v_add_f32 v99, 1.0, v99\n\t"
        "v_add_f32 v100, 1.0, v100\n\t"
        "v_add_f32 v101, 1.0, v101\n\t"
        "v_add_f32 v102, 1.0, v102\n\t"
        "v_add_f32 v103, 1.0, v103\n\t"
        "v_add_f32 v104, 1.0, v104\n\t"
        "v_add_f32 v105, 1.0, v105\n\t"
        "v_add_f32 v106, 1.0, v106\n\t"
        "v_add_f32 v107, 1.0, v107\n\t"
        "v_add_f32 v108, 1.0, v108\n\t"
        "v_add_f32 v109, 1.0, v109\n\t"
        "v_add_f32 v110, 1.0, v110\n\t"
        "v_add_f32 v111, 1.0, v111\n\t"
        "v_rcp_f32 v80, v80\n\t"
        "v_rcp_f32 v81, v81\n\t"
        "v_rcp_f32 v82, v82\n\t"
        "v_rcp_f32 v83, v83\n\t"
        "v_rcp_f32 v84, v84\n\t"
        "v_rcp_f32 v85, v85\n\t"
        "v_rcp_f32 v86, v86\n\t"
        "v_rcp_f32 v87, v87\n\t"
        "v_rcp_f32 v88, v88\n\t"
        "v_rcp_f32 v89, v89\n\t"
        "v_rcp_f32 v90, v90\n\t"
        "v_rcp_f32 v91, v91\n\t"
        "v_rcp_f32 v92, v92\n\t"
        "v_rcp_f32 v93, v93\n\t"
        "v_rcp_f32 v94, v94\n\t"
        "v_rcp_f32 v95, v95\n\t"
        "v_rcp_f32 v96, v96\n\t"
        "v_rcp_f32 v97, v97\n\t"
        "v_rcp_f32 v98, v98\n\t"
        "v_rcp_f32 v99, v99\n\t"
        "v_rcp_f32 v100, v100\n\t"
        "v_rcp_f32 v101, v101\n\t"
        "v_rcp_f32 v102, v102\n\t"
        "v_rcp_f32 v103, v103\n\t"
        "v_rcp_f32 v104, v104\n\t"
        "v_rcp_f32 v105, v105\n\t"
        "v_rcp_f32 v106, v106\n\t"
        "v_rcp_f32 v107, v107\n\t"
        "v_rcp_f32 v108, v108\n\t"
        "v_rcp_f32 v109, v109\n\t"
        "v_rcp_f32 v110, v110\n\t"
        "v_rcp_f32 v111, v111\n\t"
        "v_mul_f32 v80, v80, v16\n\t"
        "v_mul_f32 v81, v81, v17\n\t"
        "v_mul_f32 v82, v82, v18\n\t"
        "v_mul_f32 v83, v83, v19\n\t"
        "v_mul_f32 v84, v84, v20\n\t"
        "v_mul_f32 v85, v85, v21\n\t"
        "v_mul_f32 v86, v86, v22\n\t"
        "v_mul_f32 v87, v87, v23\n\t"
        "v_mul_f32 v88, v88, v24\n\t"
        "v_mul_f32 v89, v89, v25\n\t"
        "v_mul_f32 v90, v90, v26\n\t"
        "v_mul_f32 v91, v91, v27\n\t"
        "v_mul_f32 v92, v92, v28\n\t"
        "v_mul_f32 v93, v93, v29\n\t"
        "v_mul_f32 v94, v94, v30\n\t"
        "v_mul_f32 v95, v95, v31\n\t"
        "v_mul_f32 v96, v96, v32\n\t"
        "v_mul_f32 v97, v97, v33\n\t"
        "v_mul_f32 v98, v98, v34\n\t"
        "v_mul_f32 v99, v99, v35\n\t"
        "v_mul_f32 v100, v100, v36\n\t"
        "v_mul_f32 v101, v101, v37\n\t"
        "v_mul_f32 v102, v102, v38\n\t"
        "v_mul_f32 v103, v103, v39\n\t"
        "v_mul_f32 v104, v104, v40\n\t"
        "v_mul_f32 v105, v105, v41\n\t"
        "v_mul_f32 v106, v106, v42\n\t"
        "v_mul_f32 v107, v107, v43\n\t"
        "v_mul_f32 v108, v108, v44\n\t"
        "v_mul_f32 v109, v109, v45\n\t"
        "v_mul_f32 v110, v110, v46\n\t"
        "v_mul_f32 v111, v111, v47\n\t"
        "v_cvt_pk_bf16_f32 v208, v80, v81\n\t"
        "v_cvt_pk_bf16_f32 v209, v82, v83\n\t"
        "v_cvt_pk_bf16_f32 v210, v84, v85\n\t"
        "v_cvt_pk_bf16_f32 v211, v86, v87\n\t"
        "v_cvt_pk_bf16_f32 v212, v88, v89\n\t"
        "v_cvt_pk_bf16_f32 v213, v90, v91\n\t"
        "v_cvt_pk_bf16_f32 v214, v92, v93\n\t"
        "v_cvt_pk_bf16_f32 v215, v94, v95\n\t"
        "v_cvt_pk_bf16_f32 v216, v96, v97\n\t"
        "v_cvt_pk_bf16_f32 v217, v98, v99\n\t"
        "v_cvt_pk_bf16_f32 v218, v100, v101\n\t"
        "v_cvt_pk_bf16_f32 v219, v102, v103\n\t"
        "v_cvt_pk_bf16_f32 v220, v104, v105\n\t"
        "v_cvt_pk_bf16_f32 v221, v106, v107\n\t"
        "v_cvt_pk_bf16_f32 v222, v108, v109\n\t"
        "v_cvt_pk_bf16_f32 v223, v110, v111\n\t"
                : "={v[208:211]}"(hv[0]), "={v[212:215]}"(hv[1]), "={v[216:219]}"(hv[2]), "={v[220:223]}"(hv[3]), "+{v[80:95]}"(st0), "+{v[96:111]}"(st1)
                : "{v[16:31]}"(c0), "{v[32:47]}"(c1));

            } else {
            asm volatile(
        "v_mov_b32 v120, 0\n\t"
        "v_mov_b32 v121, 0\n\t"
        "v_mov_b32 v122, 0\n\t"
        "v_mov_b32 v123, 0\n\t"
        "v_mov_b32 v124, %[one]\n\t"
        "v_mov_b32 v125, %[one]\n\t"
        "v_mov_b64 v[126:127], v[124:125]\n\t"
        "v_mov_b64 v[128:129], v[124:125]\n\t"
        "v_mov_b64 v[130:131], v[124:125]\n\t"
        "v_mov_b64 v[132:133], v[124:125]\n\t"
        "v_mov_b64 v[134:135], v[124:125]\n\t"
        "v_mov_b64 v[136:137], v[124:125]\n\t"
        "v_mov_b64 v[138:139], v[124:125]\n\t"
        "v_mfma_f32_32x32x16_bf16 v[48:63], v[120:123], v[120:123], 0\n\t"
        "v_mfma_f32_32x32x16_bf16 v[64:79], v[120:123], v[120:123], 0\n\t"
        "v_mfma_f32_32x32x16_bf16 v[144:159], v[120:123], v[120:123], 0\n\t"
        "v_mfma_f32_32x32x16_bf16 v[160:175], v[120:123], v[120:123], 0\n\t"
        "v_mfma_f32_32x32x16_bf16 v[176:191], v[120:123], v[120:123], 0\n\t"
        "v_mfma_f32_32x32x16_bf16 v[192:207], v[120:123], v[120:123], 0\n\t"
                : "={v[48:63]}"(o0), "={v[64:79]}"(o1), "=&{v[124:139]}"(vv), "={v[144:159]}"(c00), "={v[160:175]}"(c01), "={v[176:191]}"(c10), "={v[192:207]}"(c11), "+{v142}"(goff), "+v"(ooff)
                : [one] "v"(one)
                : "v80", "v81", "v82", "v83", "v84", "v85", "v86", "v87", "v88", "v89", "v90", "v91", "v92", "v93", "v94", "v95", "v96", "v97", "v98", "v99", "v100", "v101", "v102", "v103", "v104", "v105", "v106", "v107", "v108", "v109", "v110", "v111", "v120", "v121", "v122", "v123");

            }
        } else {
            asm volatile(
        "v_mov_b32 v120, 0\n\t"
        "v_mov_b32 v121, 0\n\t"
        "v_mov_b32 v122, 0\n\t"
        "v_mov_b32 v123, 0\n\t"
        "v_mov_b32 v124, %[one]\n\t"
        "v_mov_b32 v125, %[one]\n\t"
        "v_mov_b64 v[126:127], v[124:125]\n\t"
        "v_mov_b64 v[128:129], v[124:125]\n\t"
        "v_mov_b64 v[130:131], v[124:125]\n\t"
        "v_mov_b64 v[132:133], v[124:125]\n\t"
        "v_mov_b64 v[134:135], v[124:125]\n\t"
        "v_mov_b64 v[136:137], v[124:125]\n\t"
        "v_mov_b64 v[138:139], v[124:125]\n\t"
        "v_mul_f32 v96, %[nl], v32\n\t"
        "v_mul_f32 v97, %[nl], v33\n\t"
        "v_mul_f32 v98, %[nl], v34\n\t"
        "v_mul_f32 v99, %[nl], v35\n\t"
        "v_mul_f32 v100, %[nl], v36\n\t"
        "v_mul_f32 v101, %[nl], v37\n\t"
        "v_mul_f32 v102, %[nl], v38\n\t"
        "v_mul_f32 v103, %[nl], v39\n\t"
        "v_mul_f32 v104, %[nl], v40\n\t"
        "v_mul_f32 v105, %[nl], v41\n\t"
        "v_mul_f32 v106, %[nl], v42\n\t"
        "v_mul_f32 v107, %[nl], v43\n\t"
        "v_mul_f32 v108, %[nl], v44\n\t"
        "v_mul_f32 v109, %[nl], v45\n\t"
        "v_mul_f32 v110, %[nl], v46\n\t"
        "v_mul_f32 v111, %[nl], v47\n\t"
        "v_exp_f32 v96, v96\n\t"
        "v_exp_f32 v97, v97\n\t"
        "v_exp_f32 v98, v98\n\t"
        "v_exp_f32 v99, v99\n\t"
        "v_exp_f32 v100, v100\n\t"
        "v_exp_f32 v101, v101\n\t"
        "v_exp_f32 v102, v102\n\t"
        "v_exp_f32 v103, v103\n\t"
        "v_exp_f32 v104, v104\n\t"
        "v_exp_f32 v105, v105\n\t"
        "v_exp_f32 v106, v106\n\t"
        "v_exp_f32 v107, v107\n\t"
        "v_exp_f32 v108, v108\n\t"
        "v_exp_f32 v109, v109\n\t"
        "v_exp_f32 v110, v110\n\t"
        "v_exp_f32 v111, v111\n\t"
        "v_add_f32 v96, 1.0, v96\n\t"
        "v_add_f32 v97, 1.0, v97\n\t"
        "v_add_f32 v98, 1.0, v98\n\t"
        "v_add_f32 v99, 1.0, v99\n\t"
        "v_add_f32 v100, 1.0, v100\n\t"
        "v_add_f32 v101, 1.0, v101\n\t"
        "v_add_f32 v102, 1.0, v102\n\t"
        "v_add_f32 v103, 1.0, v103\n\t"
        "v_add_f32 v104, 1.0, v104\n\t"
        "v_add_f32 v105, 1.0, v105\n\t"
        "v_add_f32 v106, 1.0, v106\n\t"
        "v_add_f32 v107, 1.0, v107\n\t"
        "v_add_f32 v108, 1.0, v108\n\t"
        "v_add_f32 v109, 1.0, v109\n\t"
        "v_add_f32 v110, 1.0, v110\n\t"
        "v_add_f32 v111, 1.0, v111\n\t"
        "v_mfma_f32_32x32x16_bf16 v[48:63], v[120:123], v[120:123], 0\n\t"
        "v_rcp_f32 v96, v96\n\t"
        "v_rcp_f32 v97, v97\n\t"
        "v_rcp_f32 v98, v98\n\t"
        "v_rcp_f32 v99, v99\n\t"
        "v_mfma_f32_32x32x16_bf16 v[64:79], v[120:123], v[120:123], 0\n\t"
        "v_rcp_f32 v100, v100\n\t"
        "v_rcp_f32 v101, v101\n\t"
        "v_rcp_f32 v102, v102\n\t"
        "v_rcp_f32 v103, v103\n\t"
        "v_mfma_f32_32x32x16_bf16 v[144:159], v[120:123], v[120:123], 0\n\t"
        "v_rcp_f32 v104, v104\n\t"
        "v_rcp_f32 v105, v105\n\t"
        "v_rcp_f32 v106, v106\n\t"
        "v_rcp_f32 v107, v107\n\t"
        "v_mfma_f32_32x32x16_bf16 v[160:175], v[120:123], v[120:123], 0\n\t"
        "v_rcp_f32 v108, v108\n\t"
        "v_rcp_f32 v109, v109\n\t"
        "v_rcp_f32 v110, v110\n\t"
        "v_rcp_f32 v111, v111\n\t"
        "v_mfma_f32_32x32x16_bf16 v[176:191], v[120:123], v[120:123], 0\n\t"
        "v_mul_f32 v96, v96, v32\n\t"
        "v_mul_f32 v97, v97, v33\n\t"
        "v_mul_f32 v98, v98, v34\n\t"
        "v_mul_f32 v99, v99, v35\n\t"
        "v_mul_f32 v100, v100, v36\n\t"
        "v_mul_f32 v101, v101, v37\n\t"
        "v_mul_f32 v102, v102, v38\n\t"
        "v_mul_f32 v103, v103, v39\n\t"
        "v_mfma_f32_32x32x16_bf16 v[192:207], v[120:123], v[120:123], 0\n\t"
        "v_mul_f32 v104, v104, v40\n\t"
        "v_mul_f32 v105, v105, v41\n\t"
        "v_mul_f32 v106, v106, v42\n\t"
        "v_mul_f32 v107, v107, v43\n\t"
        "v_mul_f32 v108, v108, v44\n\t"
        "v_mul_f32 v109, v109, v45\n\t"
        "v_mul_f32 v110, v110, v46\n\t"
        "v_mul_f32 v111, v111, v47\n\t"
        "v_cvt_pk_bf16_f32 v216, v96, v97\n\t"
        "v_cvt_pk_bf16_f32 v217, v98, v99\n\t"
        "v_cvt_pk_bf16_f32 v218, v100, v101\n\t"
        "v_cvt_pk_bf16_f32 v219, v102, v103\n\t"
        "v_cvt_pk_bf16_f32 v220, v104, v105\n\t"
        "v_cvt_pk_bf16_f32 v221, v106, v107\n\t"
        "v_cvt_pk_bf16_f32 v222, v108, v109\n\t"
        "v_cvt_pk_bf16_f32 v223, v110, v111\n\t"
                : "={v[216:219]}"(hv[2]), "={v[220:223]}"(hv[3]), "={v[48:63]}"(o0), "={v[64:79]}"(o1), "=&{v[124:139]}"(vv), "={v[144:159]}"(c00), "={v[160:175]}"(c01), "={v[176:191]}"(c10), "={v[192:207]}"(c11), "+{v142}"(goff), "+v"(ooff)
                : "{v[32:47]}"(c1), [nl] "s"(0xbfb8aa3bu), [one] "v"(one)
                : "v80", "v81", "v82", "v83", "v84", "v85", "v86", "v87", "v88", "v89", "v90", "v91", "v92", "v93", "v94", "v95", "v96", "v97", "v98", "v99", "v100", "v101", "v102", "v103", "v104", "v105", "v106", "v107", "v108", "v109", "v110", "v111", "v120", "v121", "v122", "v123");

        }
        if (!ch) {
            if (!rdy) {
#pragma unroll 1
                for (int spin = 0; spin < (1 << 18); ++spin) {
                    const u32x2 fa = __builtin_amdgcn_raw_buffer_load_b64(ws, fo, 0, SC1);
                    if (__all(fa[0] == tag[0] && fa[1] == tag[1])) break;
                    asm volatile("s_sleep 2" ::: "memory");
                }
                kv_issue();
            }
            if (full2) { st16(ws, ho, hv[0]); st16(ws, ho + 2048, hv[1]); st16(ws, ho + 4096, hv[2]); st16(ws, ho + 6144, hv[3]); asm volatile("s_waitcnt vmcnt(%0)" :: "i"(4 + KV1 - KV0) : "memory"); }
            else blk_store();
        } else {
            st16(ws, ho + 4096, hv[2]); st16(ws, ho + 6144, hv[3]);
        }
    }
    f32x16 ga, gb, xa_, xb_; bf16x8 q1;
    float go, g0, g1;
    unsigned cnt; unsigned long long t64;
    {
        unsigned sa = WS_A + (unsigned)(rp * NKS + 32 + w * 14) * 2048, sw = WS_W + (unsigned)(cp * NKS + 32 + w * 14) * 2048, stg, t32;
        unsigned kvs = WS_KV + (unsigned)head * 131072 + KV1 * 4096 + w * 1024;
        const unsigned taglo = tag[0], taghi = tag[1];
    asm volatile(
        "v_mov_b32 v96, %[lcnt]\n\t"
        "v_mov_b32 v97, 1\n\t"
        "v_mov_b32 v98, %[taglo]\n\t"
        "v_mov_b32 v99, %[taghi]\n\t"
        "v_add_u32 v102, %[ql], v142\n\t"
        "v_xor_b32 v103, 0x400, v102\n\t"
        "v_add_u32 v108, %[l0], v142\n\t"
        "v_add_u32 v141, %[kl], v142\n\t"
        "v_mov_b32 v140, 0xdddd\n\t"
        "s_movk_i32 %[stg], 0\n\t"
        "s_mov_b32 m0, %[kvl]\n\t"
        "s_waitcnt lgkmcnt(0)\n\t"
        "s_cmp_lg_u32 %[kl], %[l0]\n\t"
        "s_cbranch_scc1 Lnb_%=\n\t"
        "s_branch Lbar_%=\n\t"
        "Lc0_%=:\n\t"
        "s_mov_b32 %[t32], 0x100000\n\t"
        "Lc0r_%=:\n\t"
        "ds_read_b32 v101, v96 offset:8\n\t"
        "s_waitcnt lgkmcnt(0)\n\t"
        "v_readfirstlane_b32 %[cnt], v101\n\t"
        "s_cmp_eq_u32 %[cnt], 8\n\t"
        "s_cbranch_scc1 Lc0d_%=\n\t"
        "s_sleep 1\n\t"
        "s_addk_i32 %[t32], -1\n\t"
        "s_cmp_lg_u32 %[t32], 0\n\t"
        "s_cbranch_scc1 Lc0r_%=\n\t"
        "s_branch Lc0d_%=\n\t"
        "Lpoll_%=:\n\t"
        "s_mov_b32 %[t32], 0x40000\n\t"
        "Lpr_%=:\n\t"
        "buffer_load_dwordx2 v[208:209], v102, %[rs], %[stg] offen sc1\n\t"
        "s_waitcnt vmcnt(0)\n\t"
        "v_cmp_ne_u64 vcc, v[208:209], v[98:99]\n\t"
        "s_cbranch_vccz Lgs_%=\n\t"
        "s_sleep 4\n\t"
        "s_addk_i32 %[t32], -1\n\t"
        "s_cmp_lg_u32 %[t32], 0\n\t"
        "s_cbranch_scc1 Lpr_%=\n\t"
        "s_branch Lgs_%=\n\t"
        "Lw4_%=:\n\t"
        "s_mov_b32 %[t32], 0x100000\n\t"
        "Lw4r_%=:\n\t"
        "ds_read_b32 v101, v96 offset:4\n\t"
        "s_waitcnt lgkmcnt(0)\n\t"
        "v_readfirstlane_b32 %[cnt], v101\n\t"
        "s_cmp_eq_u32 %[cnt], 4\n\t"
        "s_cbranch_scc1 Lw4d_%=\n\t"
        "s_sleep 1\n\t"
        "s_addk_i32 %[t32], -1\n\t"
        "s_cmp_lg_u32 %[t32], 0\n\t"
        "s_cbranch_scc1 Lw4r_%=\n\t"
        "s_branch Lw4d_%=\n\t"
        "Lep_%=:\n\t"
        "s_mov_b32 %[t32], %[we]\n\t"
        "s_mov_b64 exec, 1\n\t"
        "buffer_store_dwordx2 v[98:99], off, %[rs], %[t32] sc1\n\t"
        "s_mov_b64 exec, -1\n\t"
        "s_branch Lne_%=\n\t"
        "Lck_%=:\n\t"
        "s_mov_b32 %[t32], 0x100000\n\t"
        "Lckr_%=:\n\t"
        "ds_read_b32 v101, v96 offset:0\n\t"
        "s_waitcnt lgkmcnt(0)\n\t"
        "v_readfirstlane_b32 %[cnt], v101\n\t"
        "s_cmp_eq_u32 %[cnt], 8\n\t"
        "s_cbranch_scc1 Lckd_%=\n\t"
        "s_sleep 1\n\t"
        "s_addk_i32 %[t32], -1\n\t"
        "s_cmp_lg_u32 %[t32], 0\n\t"
        "s_cbranch_scc1 Lckr_%=\n\t"
        "s_branch Lckd_%=\n\t"
        "Luns_%=:\n\t"
        "s_waitcnt vmcnt(0)\n\t"
        "s_branch Lsafe_%=\n\t"
        "Lbar_%=:\n\t"
        "s_barrier\n\t"
        "s_setprio 2\n\t"
        "Lnb_%=:\n\t"
        "ds_read_b128 v[104:107], v108\n\t"
        "ds_read_b128 v[112:115], v103\n\t"
        "ds_read_b128 v[116:119], v102\n\t"
        "ds_read_b128 v[120:123], v141\n\t"
        "s_mov_b64 exec, %[vm]\n\t"
        "ds_read_b128 v[124:127], v141 offset:1024\n\t"
        "ds_read_b128 v[128:131], v141 offset:1280\n\t"
        "s_mov_b64 exec, -1\n\t"
        "s_waitcnt lgkmcnt(4)\n\t"
        "v_mfma_f32_32x32x16_bf16 v[16:31], v[104:107], v[112:115], 0\n\t"
        "s_waitcnt lgkmcnt(3)\n\t"
        "v_mfma_f32_32x32x16_bf16 v[32:47], v[104:107], v[116:119], 0\n\t"
        "s_nop 9\n\t"
        "v_max3_f32 v16, v16, v17, v18\n\t"
        "v_max3_f32 v19, v19, v20, v21\n\t"
        "v_max3_f32 v22, v22, v23, v24\n\t"
        "v_max3_f32 v25, v25, v26, v27\n\t"
        "v_max3_f32 v28, v28, v29, v30\n\t"
        "v_max3_f32 v32, v32, v33, v34\n\t"
        "v_max3_f32 v35, v35, v36, v37\n\t"
        "v_max3_f32 v38, v38, v39, v40\n\t"
        "v_max3_f32 v41, v41, v42, v43\n\t"
        "v_max3_f32 v44, v44, v45, v46\n\t"
        "v_max3_f32 v16, v16, v19, v22\n\t"
        "v_max3_f32 v25, v25, v28, v31\n\t"
        "v_max3_f32 v32, v32, v35, v38\n\t"
        "v_max3_f32 v41, v41, v44, v47\n\t"
        "v_max_f32 v16, v16, v25\n\t"
        "v_max_f32 v32, v32, v41\n\t"
        "v_mov_b32 v17, v16\n\t"
        "v_mov_b32 v33, v32\n\t"
        "v_mov_b32 v40, 0\n\t"
        "v_mov_b32 v41, 0\n\t"
        "v_permlane32_swap_b32 v16, v17\n\t"
        "v_permlane32_swap_b32 v32, v33\n\t"
        "v_mov_b64 v[42:43], v[40:41]\n\t"
        "v_mov_b64 v[44:45], v[40:41]\n\t"
        "v_mov_b64 v[46:47], v[40:41]\n\t"
        "v_max_f32 v16, v16, v17\n\t"
        "v_max_f32 v32, v32, v33\n\t"
        "v_max_f32 v80, -v16, -v32\n\t"
        "v_max_f32 v81, -v16, -v32\n\t"
        "v_mov_b64 v[82:83], v[80:81]\n\t"
        "v_mov_b64 v[84:85], v[80:81]\n\t"
        "v_mov_b64 v[86:87], v[80:81]\n\t"
        "v_mov_b64 v[88:89], v[80:81]\n\t"
        "v_mov_b64 v[90:91], v[80:81]\n\t"
        "v_mov_b64 v[92:93], v[80:81]\n\t"
        "v_mov_b64 v[94:95], v[80:81]\n\t"
        "s_waitcnt vmcnt(0) lgkmcnt(0)\n\t"
        "s_mov_b64 exec, 1\n\t"
        "ds_add_rtn_u32 v101, v96, v97 offset:8\n\t"
        "s_mov_b64 exec, -1\n\t"
        "v_mfma_f32_32x32x16_bf16 v[16:31], v[120:123], v[112:115], v[80:95]\n\t"
        "s_waitcnt lgkmcnt(0)\n\t"
        "v_readfirstlane_b32 %[cnt], v101\n\t"
        "s_mov_b64 exec, 1\n\t"
        "s_cmp_eq_u32 %[cnt], 7\n\t"
        "s_cselect_b32 %[t32], %[fh], %[oob]\n\t"
        "buffer_store_dwordx2 v[98:99], off, %[rs], %[t32] sc1\n\t"
        "s_mov_b64 exec, -1\n\t"
        "s_nop 5\n\t"
        "v_exp_f32 v16, v16\n\t"
        "v_exp_f32 v17, v17\n\t"
        "v_smfmac_f32_32x32x32_bf16 v[64:79], v[136:139], v[40:47], v140\n\t"
        "s_mov_b64 exec, %[vm]\n\t"
        "ds_read_b128 v[132:135], v141 offset:5120\n\t"
        "ds_read_b128 v[136:139], v141 offset:5376\n\t"
        "s_mov_b64 exec, -1\n\t"
        "buffer_load_dwordx4 v142, %[rs], %[kvs] offen sc1 lds\n\t"
        "s_add_u32 m0, m0, 0x2000\n\t"
        "s_addk_i32 %[kvs], 0x2000\n\t"
        "v_exp_f32 v18, v18\n\t"
        "v_exp_f32 v19, v19\n\t"
        "v_exp_f32 v20, v20\n\t"
        "v_exp_f32 v21, v21\n\t"
        "v_mfma_f32_32x32x16_bf16 v[32:47], v[120:123], v[116:119], v[80:95]\n\t"
        "buffer_load_dwordx4 v142, %[rs], %[kvs] offen sc1 lds\n\t"
        "s_add_u32 m0, m0, 0x2000\n\t"
        "s_addk_i32 %[kvs], 0x2000\n\t"
        "v_exp_f32 v22, v22\n\t"
        "v_exp_f32 v23, v23\n\t"
        "v_exp_f32 v24, v24\n\t"
        "v_exp_f32 v25, v25\n\t"
        "v_smfmac_f32_32x32x32_bf16 v[48:63], v[124:127], v[16:23], v140\n\t"
        "ds_read_b128 v[120:123], v141 offset:4096\n\t"
        "buffer_load_dwordx4 v142, %[rs], %[kvs] offen sc1 lds\n\t"
        "s_add_u32 m0, m0, 0x2000\n\t"
        "s_addk_i32 %[kvs], 0x2000\n\t"
        "v_exp_f32 v26, v26\n\t"
        "v_exp_f32 v27, v27\n\t"
        "v_exp_f32 v28, v28\n\t"
        "v_exp_f32 v29, v29\n\t"
        "v_exp_f32 v30, v30\n\t"
        "v_exp_f32 v31, v31\n\t"
        "v_exp_f32 v32, v32\n\t"
        "v_exp_f32 v33, v33\n\t"
        "v_smfmac_f32_32x32x32_bf16 v[48:63], v[128:131], v[24:31], v140\n\t"
        "s_waitcnt lgkmcnt(0)\n\t"
        "buffer_load_dwordx4 v142, %[rs], %[kvs] offen sc1 lds\n\t"
        "s_add_u32 m0, m0, 0x2000\n\t"
        "s_addk_i32 %[kvs], 0x2000\n\t"
        "v_exp_f32 v34, v34\n\t"
        "v_exp_f32 v35, v35\n\t"
        "v_exp_f32 v36, v36\n\t"
        "v_exp_f32 v37, v37\n\t"
        "v_mfma_f32_32x32x16_bf16 v[16:31], v[120:123], v[112:115], v[80:95]\n\t"
        "buffer_load_dwordx4 v142, %[rs], %[kvs] offen sc1 lds\n\t"
        "s_add_u32 m0, m0, 0x2000\n\t"
        "s_addk_i32 %[kvs], 0x2000\n\t"
        "v_exp_f32 v38, v38\n\t"
        "v_exp_f32 v39, v39\n\t"
        "v_exp_f32 v40, v40\n\t"
        "v_exp_f32 v41, v41\n\t"
        "v_smfmac_f32_32x32x32_bf16 v[64:79], v[124:127], v[32:39], v140\n\t"
        "s_nop 0\n\t"
        "buffer_load_dwordx4 v142, %[rs], %[kvs] offen sc1 lds\n\t"
        "s_add_u32 m0, m0, 0x2000\n\t"
        "s_addk_i32 %[kvs], 0x2000\n\t"
        "v_exp_f32 v42, v42\n\t"
        "v_exp_f32 v43, v43\n\t"
        "v_exp_f32 v44, v44\n\t"
        "v_exp_f32 v45, v45\n\t"
        "v_exp_f32 v46, v46\n\t"
        "v_exp_f32 v47, v47\n\t"
        "v_exp_f32 v16, v16\n\t"
        "v_exp_f32 v17, v17\n\t"
        "v_smfmac_f32_32x32x32_bf16 v[64:79], v[128:131], v[40:47], v140\n\t"
        "s_mov_b64 exec, %[vm]\n\t"
        "ds_read_b128 v[124:127], v141 offset:9216\n\t"
        "ds_read_b128 v[128:131], v141 offset:9472\n\t"
        "s_mov_b64 exec, -1\n\t"
        "buffer_load_dwordx4 v142, %[rs], %[kvs] offen sc1 lds\n\t"
        "s_add_u32 m0, m0, 0x2000\n\t"
        "s_addk_i32 %[kvs], 0x2000\n\t"
        "v_exp_f32 v18, v18\n\t"
        "v_exp_f32 v19, v19\n\t"
        "v_exp_f32 v20, v20\n\t"
        "v_exp_f32 v21, v21\n\t"
        "v_mfma_f32_32x32x16_bf16 v[32:47], v[120:123], v[116:119], v[80:95]\n\t"
        "buffer_load_dwordx4 v142, %[rs], %[kvs] offen sc1 lds\n\t"
        "s_add_u32 m0, m0, 0x2000\n\t"
        "s_addk_i32 %[kvs], 0x2000\n\t"
        "v_exp_f32 v22, v22\n\t"
        "v_exp_f32 v23, v23\n\t"
        "v_exp_f32 v24, v24\n\t"
        "v_exp_f32 v25, v25\n\t"
        "v_smfmac_f32_32x32x32_bf16 v[48:63], v[132:135], v[16:23], v140\n\t"
        "ds_read_b128 v[120:123], v141 offset:8192\n\t"
        "buffer_load_dwordx4 v142, %[rs], %[kvs] offen sc1 lds\n\t"
        "s_add_u32 m0, m0, 0x2000\n\t"
        "s_addk_i32 %[kvs], 0x2000\n\t"
        "ds_read_b32 v101, v96 offset:8\n\t"
        "v_exp_f32 v26, v26\n\t"
        "v_exp_f32 v27, v27\n\t"
        "v_exp_f32 v28, v28\n\t"
        "v_exp_f32 v29, v29\n\t"
        "v_exp_f32 v30, v30\n\t"
        "v_exp_f32 v31, v31\n\t"
        "v_exp_f32 v32, v32\n\t"
        "v_exp_f32 v33, v33\n\t"
        "v_smfmac_f32_32x32x32_bf16 v[48:63], v[136:139], v[24:31], v140\n\t"
        "s_waitcnt lgkmcnt(0)\n\t"
        "buffer_load_dwordx4 v142, %[rs], %[kvs] offen sc1 lds\n\t"
        "s_add_u32 m0, m0, 0x2000\n\t"
        "s_addk_i32 %[kvs], 0x2000\n\t"
        "v_exp_f32 v34, v34\n\t"
        "v_exp_f32 v35, v35\n\t"
        "v_exp_f32 v36, v36\n\t"
        "v_exp_f32 v37, v37\n\t"
        "v_mfma_f32_32x32x16_bf16 v[16:31], v[120:123], v[112:115], v[80:95]\n\t"
        "buffer_load_dwordx4 v142, %[rs], %[kvs] offen sc1 lds\n\t"
        "s_add_u32 m0, m0, 0x2000\n\t"
        "s_addk_i32 %[kvs], 0x2000\n\t"
        "v_exp_f32 v38, v38\n\t"
        "v_exp_f32 v39, v39\n\t"
        "v_exp_f32 v40, v40\n\t"
        "v_exp_f32 v41, v41\n\t"
        "v_smfmac_f32_32x32x32_bf16 v[64:79], v[132:135], v[32:39], v140\n\t"
        "s_nop 0\n\t"
        "v_exp_f32 v42, v42\n\t"
        "v_exp_f32 v43, v43\n\t"
        "v_exp_f32 v44, v44\n\t"
        "v_exp_f32 v45, v45\n\t"
        "v_exp_f32 v46, v46\n\t"
        "v_exp_f32 v47, v47\n\t"
        "s_setprio 3\n\t"
        "v_readfirstlane_b32 %[cnt], v101\n\t"
        "s_cmp_eq_u32 %[cnt], 8\n\t"
        "s_cbranch_scc0 Lc0_%=\n\t"
        "Lc0d_%=:\n\t"
        "v_exp_f32 v16, v16\n\t"
        "v_exp_f32 v17, v17\n\t"
        "v_smfmac_f32_32x32x32_bf16 v[64:79], v[136:139], v[40:47], v140\n\t"
        "s_mov_b64 exec, %[vm]\n\t"
        "ds_read_b128 v[132:135], v141 offset:13312\n\t"
        "ds_read_b128 v[136:139], v141 offset:13568\n\t"
        "s_mov_b64 exec, -1\n\t"
        "v_exp_f32 v18, v18\n\t"
        "v_exp_f32 v19, v19\n\t"
        "v_exp_f32 v20, v20\n\t"
        "v_exp_f32 v21, v21\n\t"
        "v_mfma_f32_32x32x16_bf16 v[32:47], v[120:123], v[116:119], v[80:95]\n\t"
        "v_exp_f32 v22, v22\n\t"
        "v_exp_f32 v23, v23\n\t"
        "v_exp_f32 v24, v24\n\t"
        "v_exp_f32 v25, v25\n\t"
        "v_smfmac_f32_32x32x32_bf16 v[48:63], v[124:127], v[16:23], v140\n\t"
        "ds_read_b128 v[120:123], v141 offset:12288\n\t"
        "v_exp_f32 v26, v26\n\t"
        "v_exp_f32 v27, v27\n\t"
        "v_exp_f32 v28, v28\n\t"
        "v_exp_f32 v29, v29\n\t"
        "v_exp_f32 v30, v30\n\t"
        "v_exp_f32 v31, v31\n\t"
        "v_exp_f32 v32, v32\n\t"
        "v_exp_f32 v33, v33\n\t"
        "v_smfmac_f32_32x32x32_bf16 v[48:63], v[128:131], v[24:31], v140\n\t"
        "s_waitcnt lgkmcnt(0)\n\t"
        "v_exp_f32 v34, v34\n\t"
        "v_exp_f32 v35, v35\n\t"
        "v_exp_f32 v36, v36\n\t"
        "v_exp_f32 v37, v37\n\t"
        "v_mfma_f32_32x32x16_bf16 v[16:31], v[120:123], v[112:115], v[80:95]\n\t"
        "v_exp_f32 v38, v38\n\t"
        "v_exp_f32 v39, v39\n\t"
        "v_exp_f32 v40, v40\n\t"
        "v_exp_f32 v41, v41\n\t"
        "v_smfmac_f32_32x32x32_bf16 v[64:79], v[124:127], v[32:39], v140\n\t"
        "s_nop 0\n\t"
        "v_exp_f32 v42, v42\n\t"
        "v_exp_f32 v43, v43\n\t"
        "v_exp_f32 v44, v44\n\t"
        "v_exp_f32 v45, v45\n\t"
        "v_exp_f32 v46, v46\n\t"
        "v_exp_f32 v47, v47\n\t"
        "v_exp_f32 v16, v16\n\t"
        "v_exp_f32 v17, v17\n\t"
        "v_smfmac_f32_32x32x32_bf16 v[64:79], v[128:131], v[40:47], v140\n\t"
        "s_mov_b64 exec, %[vm]\n\t"
        "ds_read_b128 v[124:127], v141 offset:17408\n\t"
        "ds_read_b128 v[128:131], v141 offset:17664\n\t"
        "s_mov_b64 exec, -1\n\t"
        "v_exp_f32 v18, v18\n\t"
        "v_exp_f32 v19, v19\n\t"
        "v_exp_f32 v20, v20\n\t"
        "v_exp_f32 v21, v21\n\t"
        "v_mfma_f32_32x32x16_bf16 v[32:47], v[120:123], v[116:119], v[80:95]\n\t"
        "v_exp_f32 v22, v22\n\t"
        "v_exp_f32 v23, v23\n\t"
        "v_exp_f32 v24, v24\n\t"
        "v_exp_f32 v25, v25\n\t"
        "v_smfmac_f32_32x32x32_bf16 v[48:63], v[132:135], v[16:23], v140\n\t"
        "ds_read_b128 v[120:123], v141 offset:16384\n\t"
        "v_exp_f32 v26, v26\n\t"
        "v_exp_f32 v27, v27\n\t"
        "v_exp_f32 v28, v28\n\t"
        "v_exp_f32 v29, v29\n\t"
        "v_exp_f32 v30, v30\n\t"
        "v_exp_f32 v31, v31\n\t"
        "v_exp_f32 v32, v32\n\t"
        "v_exp_f32 v33, v33\n\t"
        "v_smfmac_f32_32x32x32_bf16 v[48:63], v[136:139], v[24:31], v140\n\t"
        "s_waitcnt lgkmcnt(0)\n\t"
        "v_exp_f32 v34, v34\n\t"
        "v_exp_f32 v35, v35\n\t"
        "v_exp_f32 v36, v36\n\t"
        "v_exp_f32 v37, v37\n\t"
        "v_mfma_f32_32x32x16_bf16 v[16:31], v[120:123], v[112:115], v[80:95]\n\t"
        "v_exp_f32 v38, v38\n\t"
        "v_exp_f32 v39, v39\n\t"
        "v_exp_f32 v40, v40\n\t"
        "v_exp_f32 v41, v41\n\t"
        "v_smfmac_f32_32x32x32_bf16 v[64:79], v[132:135], v[32:39], v140\n\t"
        "s_nop 0\n\t"
        "v_exp_f32 v42, v42\n\t"
        "v_exp_f32 v43, v43\n\t"
        "v_exp_f32 v44, v44\n\t"
        "v_exp_f32 v45, v45\n\t"
        "v_exp_f32 v46, v46\n\t"
        "v_exp_f32 v47, v47\n\t"
        "s_waitcnt vmcnt(0)\n\t"
        "s_setprio 2\n\t"
        "s_mov_b64 exec, 1\n\t"
        "ds_add_u32 v96, v97\n\t"
        "s_mov_b64 exec, -1\n\t"
        "v_mul_u32_u24 v102, 4, v142\n\t"
        "s_cmp_eq_u32 %[kl], %[l0]\n\t"
        "s_cselect_b32 %[stg], %[fhb], %[oob]\n\t"
        "s_cselect_b64 %[t64], -1, 0\n\t"
        "s_cselect_b32 %[cnt], 1, 0\n\t"
        "v_mov_b32 v97, %[cnt]\n\t"
        "v_exp_f32 v16, v16\n\t"
        "v_exp_f32 v17, v17\n\t"
        "v_smfmac_f32_32x32x32_bf16 v[64:79], v[136:139], v[40:47], v140\n\t"
        "s_mov_b64 exec, %[vm]\n\t"
        "ds_read_b128 v[132:135], v141 offset:21504\n\t"
        "ds_read_b128 v[136:139], v141 offset:21760\n\t"
        "s_mov_b64 exec, -1\n\t"
        "v_exp_f32 v18, v18\n\t"
        "v_exp_f32 v19, v19\n\t"
        "v_exp_f32 v20, v20\n\t"
        "v_exp_f32 v21, v21\n\t"
        "v_mfma_f32_32x32x16_bf16 v[32:47], v[120:123], v[116:119], v[80:95]\n\t"
        "v_exp_f32 v22, v22\n\t"
        "v_exp_f32 v23, v23\n\t"
        "v_exp_f32 v24, v24\n\t"
        "v_exp_f32 v25, v25\n\t"
        "v_smfmac_f32_32x32x32_bf16 v[48:63], v[124:127], v[16:23], v140\n\t"
        "ds_read_b128 v[120:123], v141 offset:20480\n\t"
        "v_exp_f32 v26, v26\n\t"
        "v_exp_f32 v27, v27\n\t"
        "v_exp_f32 v28, v28\n\t"
        "v_exp_f32 v29, v29\n\t"
        "v_exp_f32 v30, v30\n\t"
        "v_exp_f32 v31, v31\n\t"
        "v_exp_f32 v32, v32\n\t"
        "v_exp_f32 v33, v33\n\t"
        "v_smfmac_f32_32x32x32_bf16 v[48:63], v[128:131], v[24:31], v140\n\t"
        "s_waitcnt lgkmcnt(0)\n\t"
        "v_exp_f32 v34, v34\n\t"
        "v_exp_f32 v35, v35\n\t"
        "v_exp_f32 v36, v36\n\t"
        "v_exp_f32 v37, v37\n\t"
        "v_mfma_f32_32x32x16_bf16 v[16:31], v[120:123], v[112:115], v[80:95]\n\t"
        "v_exp_f32 v38, v38\n\t"
        "v_exp_f32 v39, v39\n\t"
        "v_exp_f32 v40, v40\n\t"
        "v_exp_f32 v41, v41\n\t"
        "v_smfmac_f32_32x32x32_bf16 v[64:79], v[124:127], v[32:39], v140\n\t"
        "s_nop 0\n\t"
        "v_exp_f32 v42, v42\n\t"
        "v_exp_f32 v43, v43\n\t"
        "v_exp_f32 v44, v44\n\t"
        "v_exp_f32 v45, v45\n\t"
        "v_exp_f32 v46, v46\n\t"
        "v_exp_f32 v47, v47\n\t"
        "v_exp_f32 v16, v16\n\t"
        "v_exp_f32 v17, v17\n\t"
        "v_smfmac_f32_32x32x32_bf16 v[64:79], v[128:131], v[40:47], v140\n\t"
        "s_mov_b64 exec, %[vm]\n\t"
        "ds_read_b128 v[124:127], v141 offset:25600\n\t"
        "ds_read_b128 v[128:131], v141 offset:25856\n\t"
        "s_mov_b64 exec, -1\n\t"
        "v_exp_f32 v18, v18\n\t"
        "v_exp_f32 v19, v19\n\t"
        "v_exp_f32 v20, v20\n\t"
        "v_exp_f32 v21, v21\n\t"
        "v_mfma_f32_32x32x16_bf16 v[32:47], v[120:123], v[116:119], v[80:95]\n\t"
        "v_exp_f32 v22, v22\n\t"
        "v_exp_f32 v23, v23\n\t"
        "v_exp_f32 v24, v24\n\t"
        "v_exp_f32 v25, v25\n\t"
        "v_smfmac_f32_32x32x32_bf16 v[48:63], v[132:135], v[16:23], v140\n\t"
        "ds_read_b128 v[120:123], v141 offset:24576\n\t"
        "v_exp_f32 v26, v26\n\t"
        "v_exp_f32 v27, v27\n\t"
        "v_exp_f32 v28, v28\n\t"
        "v_exp_f32 v29, v29\n\t"
        "v_exp_f32 v30, v30\n\t"
        "v_exp_f32 v31, v31\n\t"
        "v_exp_f32 v32, v32\n\t"
        "v_exp_f32 v33, v33\n\t"
        "v_smfmac_f32_32x32x32_bf16 v[48:63], v[136:139], v[24:31], v140\n\t"
        "s_waitcnt lgkmcnt(0)\n\t"
        "v_exp_f32 v34, v34\n\t"
        "v_exp_f32 v35, v35\n\t"
        "v_exp_f32 v36, v36\n\t"
        "v_exp_f32 v37, v37\n\t"
        "v_mfma_f32_32x32x16_bf16 v[16:31], v[120:123], v[112:115], v[80:95]\n\t"
        "buffer_load_dwordx2 v[208:209], v102, %[rs], %[stg] offen sc1\n\t"
        "v_exp_f32 v38, v38\n\t"
        "v_exp_f32 v39, v39\n\t"
        "v_exp_f32 v40, v40\n\t"
        "v_exp_f32 v41, v41\n\t"
        "v_smfmac_f32_32x32x32_bf16 v[64:79], v[132:135], v[32:39], v140\n\t"
        "s_nop 0\n\t"
        "v_exp_f32 v42, v42\n\t"
        "v_exp_f32 v43, v43\n\t"
        "v_exp_f32 v44, v44\n\t"
        "v_exp_f32 v45, v45\n\t"
        "v_exp_f32 v46, v46\n\t"
        "v_exp_f32 v47, v47\n\t"
        "s_waitcnt vmcnt(0)\n\t"
        "v_cmp_ne_u64 vcc, v[208:209], v[98:99]\n\t"
        "s_and_b64 vcc, vcc, %[t64]\n\t"
        "s_cbranch_scc1 Lpoll_%=\n\t"
        "Lgs_%=:\n\t"
        "s_mov_b64 exec, 1\n\t"
        "ds_add_u32 v96, v97 offset:4\n\t"
        "s_mov_b64 exec, -1\n\t"
        "ds_read_b32 v101, v96 offset:4\n\t"
        "v_bfe_u32 v15, v142, 2, 7\n\t"
        "s_waitcnt lgkmcnt(0)\n\t"
        "v_readfirstlane_b32 %[cnt], v101\n\t"
        "s_cmp_eq_u32 %[cnt], 4\n\t"
        "s_cbranch_scc0 Lw4_%=\n\t"
        "Lw4d_%=:\n\t"
        "s_cmp_eq_u32 %[eo], 0\n\t"
        "s_cbranch_scc1 Lep_%=\n\t"
        "Lne_%=:\n\t"
        "v_exp_f32 v16, v16\n\t"
        "v_exp_f32 v17, v17\n\t"
        "v_smfmac_f32_32x32x32_bf16 v[64:79], v[136:139], v[40:47], v140\n\t"
        "s_mov_b64 exec, %[vm]\n\t"
        "ds_read_b128 v[132:135], v141 offset:29696\n\t"
        "ds_read_b128 v[136:139], v141 offset:29952\n\t"
        "s_mov_b64 exec, -1\n\t"
        "buffer_load_dword v13, v15, %[rs], %[sgo] offen sc1\n\t"
        "buffer_load_dword v14, v15, %[rs], %[sga] offen sc1\n\t"
        "buffer_load_dword v15, v15, %[rs], %[sga] offen offset:128 sc1\n\t"
        "v_exp_f32 v18, v18\n\t"
        "v_exp_f32 v19, v19\n\t"
        "v_exp_f32 v20, v20\n\t"
        "v_exp_f32 v21, v21\n\t"
        "v_mfma_f32_32x32x16_bf16 v[32:47], v[120:123], v[116:119], v[80:95]\n\t"
        "buffer_load_dwordx4 v[208:211], v142, %[rs], %[sa] offen offset:0 sc1\n\t"
        "v_exp_f32 v22, v22\n\t"
        "v_exp_f32 v23, v23\n\t"
        "v_exp_f32 v24, v24\n\t"
        "v_exp_f32 v25, v25\n\t"
        "v_smfmac_f32_32x32x32_bf16 v[48:63], v[124:127], v[16:23], v140\n\t"
        "ds_read_b128 v[120:123], v141 offset:28672\n\t"
        "buffer_load_dwordx4 v[216:219], v142, %[rs], %[sw] offen offset:0 sc1\n\t"
        "v_exp_f32 v26, v26\n\t"
        "v_exp_f32 v27, v27\n\t"
        "v_exp_f32 v28, v28\n\t"
        "v_exp_f32 v29, v29\n\t"
        "v_exp_f32 v30, v30\n\t"
        "v_exp_f32 v31, v31\n\t"
        "v_exp_f32 v32, v32\n\t"
        "v_exp_f32 v33, v33\n\t"
        "v_smfmac_f32_32x32x32_bf16 v[48:63], v[128:131], v[24:31], v140\n\t"
        "s_waitcnt lgkmcnt(0)\n\t"
        "buffer_load_dwordx4 v[220:223], v142, %[rs], %[sw] offen offset:1024 sc1\n\t"
        "v_exp_f32 v34, v34\n\t"
        "v_exp_f32 v35, v35\n\t"
        "v_exp_f32 v36, v36\n\t"
        "v_exp_f32 v37, v37\n\t"
        "v_mfma_f32_32x32x16_bf16 v[16:31], v[120:123], v[112:115], v[80:95]\n\t"
        "buffer_load_dwordx4 v[212:215], v142, %[rs], %[sa] offen offset:1024 sc1\n\t"
        "v_exp_f32 v38, v38\n\t"
        "v_exp_f32 v39, v39\n\t"
        "v_exp_f32 v40, v40\n\t"
        "v_exp_f32 v41, v41\n\t"
        "v_smfmac_f32_32x32x32_bf16 v[64:79], v[124:127], v[32:39], v140\n\t"
        "s_nop 0\n\t"
        "buffer_load_dwordx4 v[224:227], v142, %[rs], %[sa] offen offset:2048 sc1\n\t"
        "v_exp_f32 v42, v42\n\t"
        "v_exp_f32 v43, v43\n\t"
        "v_exp_f32 v44, v44\n\t"
        "v_exp_f32 v45, v45\n\t"
        "v_exp_f32 v46, v46\n\t"
        "v_exp_f32 v47, v47\n\t"
        "v_exp_f32 v16, v16\n\t"
        "v_exp_f32 v17, v17\n\t"
        "v_smfmac_f32_32x32x32_bf16 v[64:79], v[128:131], v[40:47], v140\n\t"
        "s_mov_b64 exec, %[vm]\n\t"
        "ds_read_b128 v[124:127], v141 offset:33792\n\t"
        "ds_read_b128 v[128:131], v141 offset:34048\n\t"
        "s_mov_b64 exec, -1\n\t"
        "buffer_load_dwordx4 v[232:235], v142, %[rs], %[sw] offen offset:2048 sc1\n\t"
        "v_exp_f32 v18, v18\n\t"
        "v_exp_f32 v19, v19\n\t"
        "v_exp_f32 v20, v20\n\t"
        "v_exp_f32 v21, v21\n\t"
        "v_mfma_f32_32x32x16_bf16 v[32:47], v[120:123], v[116:119], v[80:95]\n\t"
        "buffer_load_dwordx4 v[236:239], v142, %[rs], %[sw] offen offset:3072 sc1\n\t"
        "v_exp_f32 v22, v22\n\t"
        "v_exp_f32 v23, v23\n\t"
        "v_exp_f32 v24, v24\n\t"
        "v_exp_f32 v25, v25\n\t"
        "v_smfmac_f32_32x32x32_bf16 v[48:63], v[132:135], v[16:23], v140\n\t"
        "ds_read_b128 v[120:123], v141 offset:32768\n\t"
        "buffer_load_dwordx4 v[228:231], v142, %[rs], %[sa] offen offset:3072 sc1\n\t"
        "ds_read_b32 v101, v96 offset:0\n\t"
        "v_exp_f32 v26, v26\n\t"
        "v_exp_f32 v27, v27\n\t"
        "v_exp_f32 v28, v28\n\t"
        "v_exp_f32 v29, v29\n\t"
        "v_exp_f32 v30, v30\n\t"
        "v_exp_f32 v31, v31\n\t"
        "v_exp_f32 v32, v32\n\t"
        "v_exp_f32 v33, v33\n\t"
        "v_smfmac_f32_32x32x32_bf16 v[48:63], v[136:139], v[24:31], v140\n\t"
        "s_waitcnt lgkmcnt(0)\n\t"
        "v_exp_f32 v34, v34\n\t"
        "v_exp_f32 v35, v35\n\t"
        "v_exp_f32 v36, v36\n\t"
        "v_exp_f32 v37, v37\n\t"
        "v_mfma_f32_32x32x16_bf16 v[16:31], v[120:123], v[112:115], v[80:95]\n\t"
        "v_exp_f32 v38, v38\n\t"
        "v_exp_f32 v39, v39\n\t"
        "v_exp_f32 v40, v40\n\t"
        "v_exp_f32 v41, v41\n\t"
        "v_smfmac_f32_32x32x32_bf16 v[64:79], v[132:135], v[32:39], v140\n\t"
        "s_nop 0\n\t"
        "v_exp_f32 v42, v42\n\t"
        "v_exp_f32 v43, v43\n\t"
        "v_exp_f32 v44, v44\n\t"
        "v_exp_f32 v45, v45\n\t"
        "v_exp_f32 v46, v46\n\t"
        "v_exp_f32 v47, v47\n\t"
        "v_add_u32 v141, 0x8000, v141\n\t"
        "s_add_u32 %[stg], %[sa], 0x6000\n\t"
        "s_add_u32 %[kvs], %[sw], 0x6000\n\t"
        "s_addk_i32 %[sa], 0x1000\n\t"
        "s_addk_i32 %[sw], 0x1000\n\t"
        "v_readfirstlane_b32 %[cnt], v101\n\t"
        "s_cmp_eq_u32 %[cnt], 8\n\t"
        "s_cbranch_scc0 Lck_%=\n\t"
        "Lckd_%=:\n\t"
        "v_exp_f32 v16, v16\n\t"
        "v_exp_f32 v17, v17\n\t"
        "v_smfmac_f32_32x32x32_bf16 v[64:79], v[136:139], v[40:47], v140\n\t"
        "s_mov_b64 exec, %[vm]\n\t"
        "ds_read_b128 v[132:135], v141 offset:5120\n\t"
        "ds_read_b128 v[136:139], v141 offset:5376\n\t"
        "s_mov_b64 exec, -1\n\t"
        "v_exp_f32 v18, v18\n\t"
        "v_exp_f32 v19, v19\n\t"
        "v_exp_f32 v20, v20\n\t"
        "v_exp_f32 v21, v21\n\t"
        "v_mfma_f32_32x32x16_bf16 v[32:47], v[120:123], v[116:119], v[80:95]\n\t"
        "v_exp_f32 v22, v22\n\t"
        "v_exp_f32 v23, v23\n\t"
        "v_exp_f32 v24, v24\n\t"
        "v_exp_f32 v25, v25\n\t"
        "v_smfmac_f32_32x32x32_bf16 v[48:63], v[124:127], v[16:23], v140\n\t"
        "ds_read_b128 v[120:123], v141 offset:4096\n\t"
        "s_waitcnt vmcnt(4)\n\t"
        "v_exp_f32 v26, v26\n\t"
        "v_exp_f32 v27, v27\n\t"
        "v_exp_f32 v28, v28\n\t"
        "v_exp_f32 v29, v29\n\t"
        "v_mfma_f32_32x32x16_bf16 v[144:159], v[216:219], v[208:211], v[144:159]\n\t"
        "v_exp_f32 v30, v30\n\t"
        "v_exp_f32 v31, v31\n\t"
        "v_exp_f32 v32, v32\n\t"
        "v_exp_f32 v33, v33\n\t"
        "v_smfmac_f32_32x32x32_bf16 v[48:63], v[128:131], v[24:31], v140\n\t"
        "s_waitcnt lgkmcnt(0)\n\t"
        "v_exp_f32 v34, v34\n\t"
        "v_exp_f32 v35, v35\n\t"
        "v_exp_f32 v36, v36\n\t"
        "v_exp_f32 v37, v37\n\t"
        "v_mfma_f32_32x32x16_bf16 v[16:31], v[120:123], v[112:115], v[80:95]\n\t"
        "v_exp_f32 v38, v38\n\t"
        "v_exp_f32 v39, v39\n\t"
        "v_exp_f32 v40, v40\n\t"
        "v_exp_f32 v41, v41\n\t"
        "v_smfmac_f32_32x32x32_bf16 v[64:79], v[124:127], v[32:39], v140\n\t"
        "v_exp_f32 v42, v42\n\t"
        "v_exp_f32 v43, v43\n\t"
        "v_exp_f32 v44, v44\n\t"
        "v_exp_f32 v45, v45\n\t"
        "v_mfma_f32_32x32x16_bf16 v[160:175], v[220:223], v[208:211], v[160:175]\n\t"
        "buffer_load_dwordx4 v[208:211], v142, %[rs], %[sa] offen offset:0 sc1\n\t"
        "v_exp_f32 v46, v46\n\t"
        "v_exp_f32 v47, v47\n\t"
        "v_exp_f32 v16, v16\n\t"
        "v_exp_f32 v17, v17\n\t"
        "v_smfmac_f32_32x32x32_bf16 v[64:79], v[128:131], v[40:47], v140\n\t"
        "s_mov_b64 exec, %[vm]\n\t"
        "ds_read_b128 v[124:127], v141 offset:9216\n\t"
        "ds_read_b128 v[128:131], v141 offset:9472\n\t"
        "s_mov_b64 exec, -1\n\t"
        "v_exp_f32 v18, v18\n\t"
        "v_exp_f32 v19, v19\n\t"
        "v_exp_f32 v20, v20\n\t"
        "v_exp_f32 v21, v21\n\t"
        "v_mfma_f32_32x32x16_bf16 v[32:47], v[120:123], v[116:119], v[80:95]\n\t"
        "v_exp_f32 v22, v22\n\t"
        "v_exp_f32 v23, v23\n\t"
        "v_exp_f32 v24, v24\n\t"
        "v_exp_f32 v25, v25\n\t"
        "v_smfmac_f32_32x32x32_bf16 v[48:63], v[132:135], v[16:23], v140\n\t"
        "ds_read_b128 v[120:123], v141 offset:8192\n\t"
        "v_exp_f32 v26, v26\n\t"
        "v_exp_f32 v27, v27\n\t"
        "v_exp_f32 v28, v28\n\t"
        "v_exp_f32 v29, v29\n\t"
        "v_mfma_f32_32x32x16_bf16 v[176:191], v[216:219], v[212:215], v[176:191]\n\t"
        "buffer_load_dwordx4 v[216:219], v142, %[rs], %[sw] offen offset:0 sc1\n\t"
        "v_exp_f32 v30, v30\n\t"
        "v_exp_f32 v31, v31\n\t"
        "v_exp_f32 v32, v32\n\t"
        "v_exp_f32 v33, v33\n\t"
        "v_smfmac_f32_32x32x32_bf16 v[48:63], v[136:139], v[24:31], v140\n\t"
        "s_waitcnt lgkmcnt(0)\n\t"
        "v_exp_f32 v34, v34\n\t"
        "v_exp_f32 v35, v35\n\t"
        "v_exp_f32 v36, v36\n\t"
        "v_exp_f32 v37, v37\n\t"
        "v_mfma_f32_32x32x16_bf16 v[16:31], v[120:123], v[112:115], v[80:95]\n\t"
        "v_exp_f32 v38, v38\n\t"
        "v_exp_f32 v39, v39\n\t"
        "v_exp_f32 v40, v40\n\t"
        "v_exp_f32 v41, v41\n\t"
        "v_smfmac_f32_32x32x32_bf16 v[64:79], v[132:135], v[32:39], v140\n\t"
        "v_exp_f32 v42, v42\n\t"
        "v_exp_f32 v43, v43\n\t"
        "v_exp_f32 v44, v44\n\t"
        "v_exp_f32 v45, v45\n\t"
        "v_mfma_f32_32x32x16_bf16 v[192:207], v[220:223], v[212:215], v[192:207]\n\t"
        "buffer_load_dwordx4 v[220:223], v142, %[rs], %[sw] offen offset:1024 sc1\n\t"
        "buffer_load_dwordx4 v[212:215], v142, %[rs], %[sa] offen offset:1024 sc1\n\t"
        "v_exp_f32 v46, v46\n\t"
        "v_exp_f32 v47, v47\n\t"
        "v_exp_f32 v16, v16\n\t"
        "v_exp_f32 v17, v17\n\t"
        "v_smfmac_f32_32x32x32_bf16 v[64:79], v[136:139], v[40:47], v140\n\t"
        "s_mov_b64 exec, %[vm]\n\t"
        "ds_read_b128 v[132:135], v141 offset:13312\n\t"
        "ds_read_b128 v[136:139], v141 offset:13568\n\t"
        "s_mov_b64 exec, -1\n\t"
        "v_exp_f32 v18, v18\n\t"
        "v_exp_f32 v19, v19\n\t"
        "v_exp_f32 v20, v20\n\t"
        "v_exp_f32 v21, v21\n\t"
        "v_mfma_f32_32x32x16_bf16 v[32:47], v[120:123], v[116:119], v[80:95]\n\t"
        "v_exp_f32 v22, v22\n\t"
        "v_exp_f32 v23, v23\n\t"
        "v_exp_f32 v24, v24\n\t"
        "v_exp_f32 v25, v25\n\t"
        "v_smfmac_f32_32x32x32_bf16 v[48:63], v[124:127], v[16:23], v140\n\t"
        "ds_read_b128 v[120:123], v141 offset:12288\n\t"
        "s_waitcnt vmcnt(4)\n\t"
        "v_exp_f32 v26, v26\n\t"
        "v_exp_f32 v27, v27\n\t"
        "v_exp_f32 v28, v28\n\t"
        "v_exp_f32 v29, v29\n\t"
        "v_mfma_f32_32x32x16_bf16 v[144:159], v[232:235], v[224:227], v[144:159]\n\t"
        "v_exp_f32 v30, v30\n\t"
        "v_exp_f32 v31, v31\n\t"
        "v_exp_f32 v32, v32\n\t"
        "v_exp_f32 v33, v33\n\t"
        "v_smfmac_f32_32x32x32_bf16 v[48:63], v[128:131], v[24:31], v140\n\t"
        "s_waitcnt lgkmcnt(0)\n\t"
        "v_exp_f32 v34, v34\n\t"
        "v_exp_f32 v35, v35\n\t"
        "v_exp_f32 v36, v36\n\t"
        "v_exp_f32 v37, v37\n\t"
        "v_mfma_f32_32x32x16_bf16 v[16:31], v[120:123], v[112:115], v[80:95]\n\t"
        "v_exp_f32 v38, v38\n\t"
        "v_exp_f32 v39, v39\n\t"
        "v_exp_f32 v40, v40\n\t"
        "v_exp_f32 v41, v41\n\t"
        "v_smfmac_f32_32x32x32_bf16 v[64:79], v[124:127], v[32:39], v140\n\t"
        "v_exp_f32 v42, v42\n\t"
        "v_exp_f32 v43, v43\n\t"
        "v_exp_f32 v44, v44\n\t"
        "v_exp_f32 v45, v45\n\t"
        "v_mfma_f32_32x32x16_bf16 v[160:175], v[236:239], v[224:227], v[160:175]\n\t"
        "buffer_load_dwordx4 v[224:227], v142, %[rs], %[sa] offen offset:2048 sc1\n\t"
        "v_exp_f32 v46, v46\n\t"
        "v_exp_f32 v47, v47\n\t"
        "v_exp_f32 v16, v16\n\t"
        "v_exp_f32 v17, v17\n\t"
        "v_smfmac_f32_32x32x32_bf16 v[64:79], v[128:131], v[40:47], v140\n\t"
        "s_mov_b64 exec, %[vm]\n\t"
        "ds_read_b128 v[124:127], v141 offset:17408\n\t"
        "ds_read_b128 v[128:131], v141 offset:17664\n\t"
        "s_mov_b64 exec, -1\n\t"
        "v_exp_f32 v18, v18\n\t"
        "v_exp_f32 v19, v19\n\t"
        "v_exp_f32 v20, v20\n\t"
        "v_exp_f32 v21, v21\n\t"
        "v_mfma_f32_32x32x16_bf16 v[32:47], v[120:123], v[116:119], v[80:95]\n\t"
        "v_exp_f32 v22, v22\n\t"
        "v_exp_f32 v23, v23\n\t"
        "v_exp_f32 v24, v24\n\t"
        "v_exp_f32 v25, v25\n\t"
        "v_smfmac_f32_32x32x32_bf16 v[48:63], v[132:135], v[16:23], v140\n\t"
        "ds_read_b128 v[120:123], v141 offset:16384\n\t"
        "v_exp_f32 v26, v26\n\t"
        "v_exp_f32 v27, v27\n\t"
        "v_exp_f32 v28, v28\n\t"
        "v_exp_f32 v29, v29\n\t"
        "v_mfma_f32_32x32x16_bf16 v[176:191], v[232:235], v[228:231], v[176:191]\n\t"
        "buffer_load_dwordx4 v[232:235], v142, %[rs], %[sw] offen offset:2048 sc1\n\t"
        "v_exp_f32 v30, v30\n\t"
        "v_exp_f32 v31, v31\n\t"
        "v_exp_f32 v32, v32\n\t"
        "v_exp_f32 v33, v33\n\t"
        "v_smfmac_f32_32x32x32_bf16 v[48:63], v[136:139], v[24:31], v140\n\t"
        "s_waitcnt lgkmcnt(0)\n\t"
        "v_exp_f32 v34, v34\n\t"
        "v_exp_f32 v35, v35\n\t"
        "v_exp_f32 v36, v36\n\t"
        "v_exp_f32 v37, v37\n\t"
        "v_mfma_f32_32x32x16_bf16 v[16:31], v[120:123], v[112:115], v[80:95]\n\t"
        "v_exp_f32 v38, v38\n\t"
        "v_exp_f32 v39, v39\n\t"
        "v_exp_f32 v40, v40\n\t"
        "v_exp_f32 v41, v41\n\t"
        "v_smfmac_f32_32x32x32_bf16 v[64:79], v[132:135], v[32:39], v140\n\t"
        "v_exp_f32 v42, v42\n\t"
        "v_exp_f32 v43, v43\n\t"
        "v_exp_f32 v44, v44\n\t"
        "v_exp_f32 v45, v45\n\t"
        "v_mfma_f32_32x32x16_bf16 v[192:207], v[236:239], v[228:231], v[192:207]\n\t"
        "buffer_load_dwordx4 v[236:239], v142, %[rs], %[sw] offen offset:3072 sc1\n\t"
        "buffer_load_dwordx4 v[228:231], v142, %[rs], %[sa] offen offset:3072 sc1\n\t"
        "s_addk_i32 %[sa], 0x1000\n\t"
        "s_addk_i32 %[sw], 0x1000\n\t"
        "v_exp_f32 v46, v46\n\t"
        "v_exp_f32 v47, v47\n\t"
        "v_exp_f32 v16, v16\n\t"
        "v_exp_f32 v17, v17\n\t"
        "v_smfmac_f32_32x32x32_bf16 v[64:79], v[136:139], v[40:47], v140\n\t"
        "s_mov_b64 exec, %[vm]\n\t"
        "ds_read_b128 v[132:135], v141 offset:21504\n\t"
        "ds_read_b128 v[136:139], v141 offset:21760\n\t"
        "s_mov_b64 exec, -1\n\t"
        "v_exp_f32 v18, v18\n\t"
        "v_exp_f32 v19, v19\n\t"
        "v_exp_f32 v20, v20\n\t"
        "v_exp_f32 v21, v21\n\t"
        "v_mfma_f32_32x32x16_bf16 v[32:47], v[120:123], v[116:119], v[80:95]\n\t"
        "v_exp_f32 v22, v22\n\t"
        "v_exp_f32 v23, v23\n\t"
        "v_exp_f32 v24, v24\n\t"
        "v_exp_f32 v25, v25\n\t"
        "v_smfmac_f32_32x32x32_bf16 v[48:63], v[124:127], v[16:23], v140\n\t"
        "ds_read_b128 v[120:123], v141 offset:20480\n\t"
        "s_waitcnt vmcnt(4)\n\t"
        "v_exp_f32 v26, v26\n\t"
        "v_exp_f32 v27, v27\n\t"
        "v_exp_f32 v28, v28\n\t"
        "v_exp_f32 v29, v29\n\t"
        "v_mfma_f32_32x32x16_bf16 v[144:159], v[216:219], v[208:211], v[144:159]\n\t"
        "v_exp_f32 v30, v30\n\t"
        "v_exp_f32 v31, v31\n\t"
        "v_exp_f32 v32, v32\n\t"
        "v_exp_f32 v33, v33\n\t"
        "v_smfmac_f32_32x32x32_bf16 v[48:63], v[128:131], v[24:31], v140\n\t"
        "s_waitcnt lgkmcnt(0)\n\t"
        "v_exp_f32 v34, v34\n\t"
        "v_exp_f32 v35, v35\n\t"
        "v_exp_f32 v36, v36\n\t"
        "v_exp_f32 v37, v37\n\t"
        "v_mfma_f32_32x32x16_bf16 v[16:31], v[120:123], v[112:115], v[80:95]\n\t"
        "buffer_load_dwordx4 v[96:99], v142, %[rs], %[stg] offen offset:0 sc1\n\t"
        "v_exp_f32 v38, v38\n\t"
        "v_exp_f32 v39, v39\n\t"
        "v_exp_f32 v40, v40\n\t"
        "v_exp_f32 v41, v41\n\t"
        "v_smfmac_f32_32x32x32_bf16 v[64:79], v[124:127], v[32:39], v140\n\t"
        "v_exp_f32 v42, v42\n\t"
        "v_exp_f32 v43, v43\n\t"
        "v_exp_f32 v44, v44\n\t"
        "v_exp_f32 v45, v45\n\t"
        "v_mfma_f32_32x32x16_bf16 v[160:175], v[220:223], v[208:211], v[160:175]\n\t"
        "buffer_load_dwordx4 v[208:211], v142, %[rs], %[sa] offen offset:0 sc1\n\t"
        "v_exp_f32 v46, v46\n\t"
        "v_exp_f32 v47, v47\n\t"
        "v_exp_f32 v16, v16\n\t"
        "v_exp_f32 v17, v17\n\t"
        "v_smfmac_f32_32x32x32_bf16 v[64:79], v[128:131], v[40:47], v140\n\t"
        "s_mov_b64 exec, %[vm]\n\t"
        "ds_read_b128 v[124:127], v141 offset:25600\n\t"
        "ds_read_b128 v[128:131], v141 offset:25856\n\t"
        "s_mov_b64 exec, -1\n\t"
        "buffer_load_dwordx4 v[104:107], v142, %[rs], %[kvs] offen offset:0 sc1\n\t"
        "v_exp_f32 v18, v18\n\t"
        "v_exp_f32 v19, v19\n\t"
        "v_exp_f32 v20, v20\n\t"
        "v_exp_f32 v21, v21\n\t"
        "v_mfma_f32_32x32x16_bf16 v[32:47], v[120:123], v[116:119], v[80:95]\n\t"
        "buffer_load_dwordx4 v[108:111], v142, %[rs], %[kvs] offen offset:1024 sc1\n\t"
        "v_exp_f32 v22, v22\n\t"
        "v_exp_f32 v23, v23\n\t"
        "v_exp_f32 v24, v24\n\t"
        "v_exp_f32 v25, v25\n\t"
        "v_smfmac_f32_32x32x32_bf16 v[48:63], v[132:135], v[16:23], v140\n\t"
        "ds_read_b128 v[120:123], v141 offset:24576\n\t"
        "v_exp_f32 v26, v26\n\t"
        "v_exp_f32 v27, v27\n\t"
        "v_exp_f32 v28, v28\n\t"
        "v_exp_f32 v29, v29\n\t"
        "v_mfma_f32_32x32x16_bf16 v[176:191], v[216:219], v[212:215], v[176:191]\n\t"
        "buffer_load_dwordx4 v[216:219], v142, %[rs], %[sw] offen offset:0 sc1\n\t"
        "v_exp_f32 v30, v30\n\t"
        "v_exp_f32 v31, v31\n\t"
        "v_exp_f32 v32, v32\n\t"
        "v_exp_f32 v33, v33\n\t"
        "v_smfmac_f32_32x32x32_bf16 v[48:63], v[136:139], v[24:31], v140\n\t"
        "s_waitcnt lgkmcnt(0)\n\t"
        "v_exp_f32 v34, v34\n\t"
        "v_exp_f32 v35, v35\n\t"
        "v_exp_f32 v36, v36\n\t"
        "v_exp_f32 v37, v37\n\t"
        "v_mfma_f32_32x32x16_bf16 v[16:31], v[120:123], v[112:115], v[80:95]\n\t"
        "buffer_load_dwordx4 v[100:103], v142, %[rs], %[stg] offen offset:1024 sc1\n\t"
        "v_exp_f32 v38, v38\n\t"
        "v_exp_f32 v39, v39\n\t"
        "v_exp_f32 v40, v40\n\t"
        "v_exp_f32 v41, v41\n\t"
        "v_smfmac_f32_32x32x32_bf16 v[64:79], v[132:135], v[32:39], v140\n\t"
        "v_exp_f32 v42, v42\n\t"
        "v_exp_f32 v43, v43\n\t"
        "v_exp_f32 v44, v44\n\t"
        "v_exp_f32 v45, v45\n\t"
        "v_mfma_f32_32x32x16_bf16 v[192:207], v[220:223], v[212:215], v[192:207]\n\t"
        "buffer_load_dwordx4 v[220:223], v142, %[rs], %[sw] offen offset:1024 sc1\n\t"
        "buffer_load_dwordx4 v[212:215], v142, %[rs], %[sa] offen offset:1024 sc1\n\t"
        "v_exp_f32 v46, v46\n\t"
        "v_exp_f32 v47, v47\n\t"
        "v_exp_f32 v16, v16\n\t"
        "v_exp_f32 v17, v17\n\t"
        "v_smfmac_f32_32x32x32_bf16 v[64:79], v[136:139], v[40:47], v140\n\t"
        "s_mov_b64 exec, %[vm]\n\t"
        "ds_read_b128 v[132:135], v141 offset:29696\n\t"
        "ds_read_b128 v[136:139], v141 offset:29952\n\t"
        "s_mov_b64 exec, -1\n\t"
        "v_exp_f32 v18, v18\n\t"
        "v_exp_f32 v19, v19\n\t"
        "v_exp_f32 v20, v20\n\t"
        "v_exp_f32 v21, v21\n\t"
        "v_mfma_f32_32x32x16_bf16 v[32:47], v[120:123], v[116:119], v[80:95]\n\t"
        "v_exp_f32 v22, v22\n\t"
        "v_exp_f32 v23, v23\n\t"
        "v_exp_f32 v24, v24\n\t"
        "v_exp_f32 v25, v25\n\t"
        "v_smfmac_f32_32x32x32_bf16 v[48:63], v[124:127], v[16:23], v140\n\t"
        "ds_read_b128 v[120:123], v141 offset:28672\n\t"
        "s_waitcnt vmcnt(8)\n\t"
        "v_exp_f32 v26, v26\n\t"
        "v_exp_f32 v27, v27\n\t"
        "v_exp_f32 v28, v28\n\t"
        "v_exp_f32 v29, v29\n\t"
        "v_mfma_f32_32x32x16_bf16 v[144:159], v[232:235], v[224:227], v[144:159]\n\t"
        "v_exp_f32 v30, v30\n\t"
        "v_exp_f32 v31, v31\n\t"
        "v_exp_f32 v32, v32\n\t"
        "v_exp_f32 v33, v33\n\t"
        "v_smfmac_f32_32x32x32_bf16 v[48:63], v[128:131], v[24:31], v140\n\t"
        "s_waitcnt lgkmcnt(0)\n\t"
        "v_exp_f32 v34, v34\n\t"
        "v_exp_f32 v35, v35\n\t"
        "v_exp_f32 v36, v36\n\t"
        "v_exp_f32 v37, v37\n\t"
        "v_mfma_f32_32x32x16_bf16 v[16:31], v[120:123], v[112:115], v[80:95]\n\t"
        "v_exp_f32 v38, v38\n\t"
        "v_exp_f32 v39, v39\n\t"
        "v_exp_f32 v40, v40\n\t"
        "v_exp_f32 v41, v41\n\t"
        "v_smfmac_f32_32x32x32_bf16 v[64:79], v[124:127], v[32:39], v140\n\t"
        "v_exp_f32 v42, v42\n\t"
        "v_exp_f32 v43, v43\n\t"
        "v_exp_f32 v44, v44\n\t"
        "v_exp_f32 v45, v45\n\t"
        "v_mfma_f32_32x32x16_bf16 v[160:175], v[236:239], v[224:227], v[160:175]\n\t"
        "buffer_load_dwordx4 v[224:227], v142, %[rs], %[sa] offen offset:2048 sc1\n\t"
        "v_exp_f32 v46, v46\n\t"
        "v_exp_f32 v47, v47\n\t"
        "v_exp_f32 v16, v16\n\t"
        "v_exp_f32 v17, v17\n\t"
        "v_smfmac_f32_32x32x32_bf16 v[64:79], v[128:131], v[40:47], v140\n\t"
        "s_mov_b64 exec, %[vm]\n\t"
        "ds_read_b128 v[124:127], v141 offset:33792\n\t"
        "ds_read_b128 v[128:131], v141 offset:34048\n\t"
        "s_mov_b64 exec, -1\n\t"
        "v_exp_f32 v18, v18\n\t"
        "v_exp_f32 v19, v19\n\t"
        "v_exp_f32 v20, v20\n\t"
        "v_exp_f32 v21, v21\n\t"
        "v_mfma_f32_32x32x16_bf16 v[32:47], v[120:123], v[116:119], v[80:95]\n\t"
        "v_exp_f32 v22, v22\n\t"
        "v_exp_f32 v23, v23\n\t"
        "v_exp_f32 v24, v24\n\t"
        "v_exp_f32 v25, v25\n\t"
        "v_smfmac_f32_32x32x32_bf16 v[48:63], v[132:135], v[16:23], v140\n\t"
        "ds_read_b128 v[120:123], v141 offset:32768\n\t"
        "v_exp_f32 v26, v26\n\t"
        "v_exp_f32 v27, v27\n\t"
        "v_exp_f32 v28, v28\n\t"
        "v_exp_f32 v29, v29\n\t"
        "v_mfma_f32_32x32x16_bf16 v[176:191], v[232:235], v[228:231], v[176:191]\n\t"
        "buffer_load_dwordx4 v[232:235], v142, %[rs], %[sw] offen offset:2048 sc1\n\t"
        "v_exp_f32 v30, v30\n\t"
        "v_exp_f32 v31, v31\n\t"
        "v_exp_f32 v32, v32\n\t"
        "v_exp_f32 v33, v33\n\t"
        "v_smfmac_f32_32x32x32_bf16 v[48:63], v[136:139], v[24:31], v140\n\t"
        "s_waitcnt lgkmcnt(0)\n\t"
        "v_exp_f32 v34, v34\n\t"
        "v_exp_f32 v35, v35\n\t"
        "v_exp_f32 v36, v36\n\t"
        "v_exp_f32 v37, v37\n\t"
        "v_mfma_f32_32x32x16_bf16 v[16:31], v[120:123], v[112:115], v[80:95]\n\t"
        "v_exp_f32 v38, v38\n\t"
        "v_exp_f32 v39, v39\n\t"
        "v_exp_f32 v40, v40\n\t"
        "v_exp_f32 v41, v41\n\t"
        "v_smfmac_f32_32x32x32_bf16 v[64:79], v[132:135], v[32:39], v140\n\t"
        "v_exp_f32 v42, v42\n\t"
        "v_exp_f32 v43, v43\n\t"
        "v_exp_f32 v44, v44\n\t"
        "v_exp_f32 v45, v45\n\t"
        "v_mfma_f32_32x32x16_bf16 v[192:207], v[236:239], v[228:231], v[192:207]\n\t"
        "buffer_load_dwordx4 v[236:239], v142, %[rs], %[sw] offen offset:3072 sc1\n\t"
        "buffer_load_dwordx4 v[228:231], v142, %[rs], %[sa] offen offset:3072 sc1\n\t"
        "s_addk_i32 %[sa], 0x1000\n\t"
        "s_addk_i32 %[sw], 0x1000\n\t"
        "v_exp_f32 v46, v46\n\t"
        "v_exp_f32 v47, v47\n\t"
        "v_add_u32 v141, 0x8000, v141\n\t"
        "v_exp_f32 v16, v16\n\t"
        "v_exp_f32 v17, v17\n\t"
        "v_smfmac_f32_32x32x32_bf16 v[64:79], v[136:139], v[40:47], v140\n\t"
        "s_mov_b64 exec, %[vm]\n\t"
        "ds_read_b128 v[132:135], v141 offset:5120\n\t"
        "ds_read_b128 v[136:139], v141 offset:5376\n\t"
        "s_mov_b64 exec, -1\n\t"
        "v_exp_f32 v18, v18\n\t"
        "v_exp_f32 v19, v19\n\t"
        "v_exp_f32 v20, v20\n\t"
        "v_exp_f32 v21, v21\n\t"
        "v_mfma_f32_32x32x16_bf16 v[32:47], v[120:123], v[116:119], v[80:95]\n\t"
        "v_exp_f32 v22, v22\n\t"
        "v_exp_f32 v23, v23\n\t"
        "v_exp_f32 v24, v24\n\t"
        "v_exp_f32 v25, v25\n\t"
        "v_smfmac_f32_32x32x32_bf16 v[48:63], v[124:127], v[16:23], v140\n\t"
        "ds_read_b128 v[120:123], v141 offset:4096\n\t"
        "s_waitcnt vmcnt(4)\n\t"
        "v_exp_f32 v26, v26\n\t"
        "v_exp_f32 v27, v27\n\t"
        "v_exp_f32 v28, v28\n\t"
        "v_exp_f32 v29, v29\n\t"
        "v_mfma_f32_32x32x16_bf16 v[144:159], v[216:219], v[208:211], v[144:159]\n\t"
        "v_exp_f32 v30, v30\n\t"
        "v_exp_f32 v31, v31\n\t"
        "v_exp_f32 v32, v32\n\t"
        "v_exp_f32 v33, v33\n\t"
        "v_smfmac_f32_32x32x32_bf16 v[48:63], v[128:131], v[24:31], v140\n\t"
        "s_waitcnt lgkmcnt(0)\n\t"
        "v_exp_f32 v34, v34\n\t"
        "v_exp_f32 v35, v35\n\t"
        "v_exp_f32 v36, v36\n\t"
        "v_exp_f32 v37, v37\n\t"
        "v_mfma_f32_32x32x16_bf16 v[16:31], v[120:123], v[112:115], v[80:95]\n\t"
        "buffer_load_dwordx4 v[240:243], v142, %[rs], %[stg] offen offset:2048 sc1\n\t"
        "v_exp_f32 v38, v38\n\t"
        "v_exp_f32 v39, v39\n\t"
        "v_exp_f32 v40, v40\n\t"
        "v_exp_f32 v41, v41\n\t"
        "v_smfmac_f32_32x32x32_bf16 v[64:79], v[124:127], v[32:39], v140\n\t"
        "v_exp_f32 v42, v42\n\t"
        "v_exp_f32 v43, v43\n\t"
        "v_exp_f32 v44, v44\n\t"
        "v_exp_f32 v45, v45\n\t"
        "v_mfma_f32_32x32x16_bf16 v[160:175], v[220:223], v[208:211], v[160:175]\n\t"
        "buffer_load_dwordx4 v[208:211], v142, %[rs], %[sa] offen offset:0 sc1\n\t"
        "v_exp_f32 v46, v46\n\t"
        "v_exp_f32 v47, v47\n\t"
        "v_exp_f32 v16, v16\n\t"
        "v_exp_f32 v17, v17\n\t"
        "v_smfmac_f32_32x32x32_bf16 v[64:79], v[128:131], v[40:47], v140\n\t"
        "s_mov_b64 exec, %[vm]\n\t"
        "ds_read_b128 v[124:127], v141 offset:9216\n\t"
        "ds_read_b128 v[128:131], v141 offset:9472\n\t"
        "s_mov_b64 exec, -1\n\t"
        "buffer_load_dwordx4 v[248:251], v142, %[rs], %[kvs] offen offset:2048 sc1\n\t"
        "v_exp_f32 v18, v18\n\t"
        "v_exp_f32 v19, v19\n\t"
        "v_exp_f32 v20, v20\n\t"
        "v_exp_f32 v21, v21\n\t"
        "v_mfma_f32_32x32x16_bf16 v[32:47], v[120:123], v[116:119], v[80:95]\n\t"
        "buffer_load_dwordx4 v[252:255], v142, %[rs], %[kvs] offen offset:3072 sc1\n\t"
        "v_exp_f32 v22, v22\n\t"
        "v_exp_f32 v23, v23\n\t"
        "v_exp_f32 v24, v24\n\t"
        "v_exp_f32 v25, v25\n\t"
        "v_smfmac_f32_32x32x32_bf16 v[48:63], v[132:135], v[16:23], v140\n\t"
        "ds_read_b128 v[120:123], v141 offset:8192\n\t"
        "v_exp_f32 v26, v26\n\t"
        "v_exp_f32 v27, v27\n\t"
        "v_exp_f32 v28, v28\n\t"
        "v_exp_f32 v29, v29\n\t"
        "v_mfma_f32_32x32x16_bf16 v[176:191], v[216:219], v[212:215], v[176:191]\n\t"
        "buffer_load_dwordx4 v[216:219], v142, %[rs], %[sw] offen offset:0 sc1\n\t"
        "v_exp_f32 v30, v30\n\t"
        "v_exp_f32 v31, v31\n\t"
        "v_exp_f32 v32, v32\n\t"
        "v_exp_f32 v33, v33\n\t"
        "v_smfmac_f32_32x32x32_bf16 v[48:63], v[136:139], v[24:31], v140\n\t"
        "s_waitcnt lgkmcnt(0)\n\t"
        "v_exp_f32 v34, v34\n\t"
        "v_exp_f32 v35, v35\n\t"
        "v_exp_f32 v36, v36\n\t"
        "v_exp_f32 v37, v37\n\t"
        "v_mfma_f32_32x32x16_bf16 v[16:31], v[120:123], v[112:115], v[80:95]\n\t"
        "buffer_load_dwordx4 v[244:247], v142, %[rs], %[stg] offen offset:3072 sc1\n\t"
        "v_exp_f32 v38, v38\n\t"
        "v_exp_f32 v39, v39\n\t"
        "v_exp_f32 v40, v40\n\t"
        "v_exp_f32 v41, v41\n\t"
        "v_smfmac_f32_32x32x32_bf16 v[64:79], v[132:135], v[32:39], v140\n\t"
        "v_exp_f32 v42, v42\n\t"
        "v_exp_f32 v43, v43\n\t"
        "v_exp_f32 v44, v44\n\t"
        "v_exp_f32 v45, v45\n\t"
        "v_mfma_f32_32x32x16_bf16 v[192:207], v[220:223], v[212:215], v[192:207]\n\t"
        "buffer_load_dwordx4 v[220:223], v142, %[rs], %[sw] offen offset:1024 sc1\n\t"
        "buffer_load_dwordx4 v[212:215], v142, %[rs], %[sa] offen offset:1024 sc1\n\t"
        "v_exp_f32 v46, v46\n\t"
        "v_exp_f32 v47, v47\n\t"
        "v_exp_f32 v16, v16\n\t"
        "v_exp_f32 v17, v17\n\t"
        "v_smfmac_f32_32x32x32_bf16 v[64:79], v[136:139], v[40:47], v140\n\t"
        "s_mov_b64 exec, %[vm]\n\t"
        "ds_read_b128 v[132:135], v141 offset:13312\n\t"
        "ds_read_b128 v[136:139], v141 offset:13568\n\t"
        "s_mov_b64 exec, -1\n\t"
        "v_exp_f32 v18, v18\n\t"
        "v_exp_f32 v19, v19\n\t"
        "v_exp_f32 v20, v20\n\t"
        "v_exp_f32 v21, v21\n\t"
        "v_mfma_f32_32x32x16_bf16 v[32:47], v[120:123], v[116:119], v[80:95]\n\t"
        "v_exp_f32 v22, v22\n\t"
        "v_exp_f32 v23, v23\n\t"
        "v_exp_f32 v24, v24\n\t"
        "v_exp_f32 v25, v25\n\t"
        "v_smfmac_f32_32x32x32_bf16 v[48:63], v[124:127], v[16:23], v140\n\t"
        "ds_read_b128 v[120:123], v141 offset:12288\n\t"
        "s_waitcnt vmcnt(8)\n\t"
        "v_exp_f32 v26, v26\n\t"
        "v_exp_f32 v27, v27\n\t"
        "v_exp_f32 v28, v28\n\t"
        "v_exp_f32 v29, v29\n\t"
        "v_mfma_f32_32x32x16_bf16 v[144:159], v[232:235], v[224:227], v[144:159]\n\t"
        "v_exp_f32 v30, v30\n\t"
        "v_exp_f32 v31, v31\n\t"
        "v_exp_f32 v32, v32\n\t"
        "v_exp_f32 v33, v33\n\t"
        "v_smfmac_f32_32x32x32_bf16 v[48:63], v[128:131], v[24:31], v140\n\t"
        "s_waitcnt lgkmcnt(0)\n\t"
        "v_exp_f32 v34, v34\n\t"
        "v_exp_f32 v35, v35\n\t"
        "v_exp_f32 v36, v36\n\t"
        "v_exp_f32 v37, v37\n\t"
        "v_mfma_f32_32x32x16_bf16 v[16:31], v[120:123], v[112:115], v[80:95]\n\t"
        "v_exp_f32 v38, v38\n\t"
        "v_exp_f32 v39, v39\n\t"
        "v_exp_f32 v40, v40\n\t"
        "v_exp_f32 v41, v41\n\t"
        "v_smfmac_f32_32x32x32_bf16 v[64:79], v[124:127], v[32:39], v140\n\t"
        "v_exp_f32 v42, v42\n\t"
        "v_exp_f32 v43, v43\n\t"
        "v_exp_f32 v44, v44\n\t"
        "v_exp_f32 v45, v45\n\t"
        "v_mfma_f32_32x32x16_bf16 v[160:175], v[236:239], v[224:227], v[160:175]\n\t"
        "buffer_load_dwordx4 v[224:227], v142, %[rs], %[sa] offen offset:2048 sc1\n\t"
        "v_exp_f32 v46, v46\n\t"
        "v_exp_f32 v47, v47\n\t"
        "v_exp_f32 v16, v16\n\t"
        "v_exp_f32 v17, v17\n\t"
        "v_smfmac_f32_32x32x32_bf16 v[64:79], v[128:131], v[40:47], v140\n\t"
        "s_mov_b64 exec, %[vm]\n\t"
        "ds_read_b128 v[124:127], v141 offset:17408\n\t"
        "ds_read_b128 v[128:131], v141 offset:17664\n\t"
        "s_mov_b64 exec, -1\n\t"
        "v_exp_f32 v18, v18\n\t"
        "v_exp_f32 v19, v19\n\t"
        "v_exp_f32 v20, v20\n\t"
        "v_exp_f32 v21, v21\n\t"
        "v_mfma_f32_32x32x16_bf16 v[32:47], v[120:123], v[116:119], v[80:95]\n\t"
        "v_exp_f32 v22, v22\n\t"
        "v_exp_f32 v23, v23\n\t"
        "v_exp_f32 v24, v24\n\t"
        "v_exp_f32 v25, v25\n\t"
        "v_smfmac_f32_32x32x32_bf16 v[48:63], v[132:135], v[16:23], v140\n\t"
        "ds_read_b128 v[120:123], v141 offset:16384\n\t"
        "v_exp_f32 v26, v26\n\t"
        "v_exp_f32 v27, v27\n\t"
        "v_exp_f32 v28, v28\n\t"
        "v_exp_f32 v29, v29\n\t"
        "v_mfma_f32_32x32x16_bf16 v[176:191], v[232:235], v[228:231], v[176:191]\n\t"
        "buffer_load_dwordx4 v[232:235], v142, %[rs], %[sw] offen offset:2048 sc1\n\t"
        "v_exp_f32 v30, v30\n\t"
        "v_exp_f32 v31, v31\n\t"
        "v_exp_f32 v32, v32\n\t"
        "v_exp_f32 v33, v33\n\t"
        "v_smfmac_f32_32x32x32_bf16 v[48:63], v[136:139], v[24:31], v140\n\t"
        "s_waitcnt lgkmcnt(0)\n\t"
        "v_exp_f32 v34, v34\n\t"
        "v_exp_f32 v35, v35\n\t"
        "v_exp_f32 v36, v36\n\t"
        "v_exp_f32 v37, v37\n\t"
        "v_mfma_f32_32x32x16_bf16 v[16:31], v[120:123], v[112:115], v[80:95]\n\t"
        "v_exp_f32 v38, v38\n\t"
        "v_exp_f32 v39, v39\n\t"
        "v_exp_f32 v40, v40\n\t"
        "v_exp_f32 v41, v41\n\t"
        "v_smfmac_f32_32x32x32_bf16 v[64:79], v[132:135], v[32:39], v140\n\t"
        "v_exp_f32 v42, v42\n\t"
        "v_exp_f32 v43, v43\n\t"
        "v_exp_f32 v44, v44\n\t"
        "v_exp_f32 v45, v45\n\t"
        "v_mfma_f32_32x32x16_bf16 v[192:207], v[236:239], v[228:231], v[192:207]\n\t"
        "buffer_load_dwordx4 v[236:239], v142, %[rs], %[sw] offen offset:3072 sc1\n\t"
        "buffer_load_dwordx4 v[228:231], v142, %[rs], %[sa] offen offset:3072 sc1\n\t"
        "s_addk_i32 %[sa], 0x1000\n\t"
        "s_addk_i32 %[sw], 0x1000\n\t"
        "v_exp_f32 v46, v46\n\t"
        "v_exp_f32 v47, v47\n\t"
        "s_setprio 1\n\t"
        "v_exp_f32 v16, v16\n\t"
        "v_exp_f32 v17, v17\n\t"
        "v_smfmac_f32_32x32x32_bf16 v[64:79], v[136:139], v[40:47], v140\n\t"
        "s_mov_b64 exec, %[vm]\n\t"
        "ds_read_b128 v[132:135], v141 offset:21504\n\t"
        "ds_read_b128 v[136:139], v141 offset:21760\n\t"
        "s_mov_b64 exec, -1\n\t"
        "v_exp_f32 v18, v18\n\t"
        "v_exp_f32 v19, v19\n\t"
        "v_exp_f32 v20, v20\n\t"
        "v_exp_f32 v21, v21\n\t"
        "v_mfma_f32_32x32x16_bf16 v[32:47], v[120:123], v[116:119], v[80:95]\n\t"
        "v_exp_f32 v22, v22\n\t"
        "v_exp_f32 v23, v23\n\t"
        "v_exp_f32 v24, v24\n\t"
        "v_exp_f32 v25, v25\n\t"
        "v_smfmac_f32_32x32x32_bf16 v[48:63], v[124:127], v[16:23], v140\n\t"
        "ds_read_b128 v[120:123], v141 offset:20480\n\t"
        "s_waitcnt vmcnt(4)\n\t"
        "v_exp_f32 v26, v26\n\t"
        "v_exp_f32 v27, v27\n\t"
        "v_exp_f32 v28, v28\n\t"
        "v_exp_f32 v29, v29\n\t"
        "v_mfma_f32_32x32x16_bf16 v[144:159], v[216:219], v[208:211], v[144:159]\n\t"
        "v_exp_f32 v30, v30\n\t"
        "v_exp_f32 v31, v31\n\t"
        "v_exp_f32 v32, v32\n\t"
        "v_exp_f32 v33, v33\n\t"
        "v_smfmac_f32_32x32x32_bf16 v[48:63], v[128:131], v[24:31], v140\n\t"
        "s_waitcnt lgkmcnt(0)\n\t"
        "v_exp_f32 v34, v34\n\t"
        "v_exp_f32 v35, v35\n\t"
        "v_exp_f32 v36, v36\n\t"
        "v_exp_f32 v37, v37\n\t"
        "v_mfma_f32_32x32x16_bf16 v[16:31], v[120:123], v[112:115], v[80:95]\n\t"
        "v_exp_f32 v38, v38\n\t"
        "v_exp_f32 v39, v39\n\t"
        "v_exp_f32 v40, v40\n\t"
        "v_exp_f32 v41, v41\n\t"
        "v_smfmac_f32_32x32x32_bf16 v[64:79], v[124:127], v[32:39], v140\n\t"
        "v_exp_f32 v42, v42\n\t"
        "v_exp_f32 v43, v43\n\t"
        "v_exp_f32 v44, v44\n\t"
        "v_exp_f32 v45, v45\n\t"
        "v_mfma_f32_32x32x16_bf16 v[160:175], v[220:223], v[208:211], v[160:175]\n\t"
        "buffer_load_dwordx4 v[208:211], v142, %[rs], %[sa] offen offset:0 sc1\n\t"
        "v_exp_f32 v46, v46\n\t"
        "v_exp_f32 v47, v47\n\t"
        "v_exp_f32 v16, v16\n\t"
        "v_exp_f32 v17, v17\n\t"
        "v_smfmac_f32_32x32x32_bf16 v[64:79], v[128:131], v[40:47], v140\n\t"
        "s_mov_b64 exec, %[vm]\n\t"
        "ds_read_b128 v[124:127], v141 offset:25600\n\t"
        "ds_read_b128 v[128:131], v141 offset:25856\n\t"
        "s_mov_b64 exec, -1\n\t"
        "v_exp_f32 v18, v18\n\t"
        "v_exp_f32 v19, v19\n\t"
        "v_exp_f32 v20, v20\n\t"
        "v_exp_f32 v21, v21\n\t"
        "v_mfma_f32_32x32x16_bf16 v[32:47], v[120:123], v[116:119], v[80:95]\n\t"
        "v_exp_f32 v22, v22\n\t"
        "v_exp_f32 v23, v23\n\t"
        "v_exp_f32 v24, v24\n\t"
        "v_exp_f32 v25, v25\n\t"
        "v_smfmac_f32_32x32x32_bf16 v[48:63], v[132:135], v[16:23], v140\n\t"
        "ds_read_b128 v[120:123], v141 offset:24576\n\t"
        "v_exp_f32 v26, v26\n\t"
        "v_exp_f32 v27, v27\n\t"
        "v_exp_f32 v28, v28\n\t"
        "v_exp_f32 v29, v29\n\t"
        "v_mfma_f32_32x32x16_bf16 v[176:191], v[216:219], v[212:215], v[176:191]\n\t"
        "buffer_load_dwordx4 v[216:219], v142, %[rs], %[sw] offen offset:0 sc1\n\t"
        "v_exp_f32 v30, v30\n\t"
        "v_exp_f32 v31, v31\n\t"
        "v_exp_f32 v32, v32\n\t"
        "v_exp_f32 v33, v33\n\t"
        "v_smfmac_f32_32x32x32_bf16 v[48:63], v[136:139], v[24:31], v140\n\t"
        "s_waitcnt lgkmcnt(0)\n\t"
        "v_exp_f32 v34, v34\n\t"
        "v_exp_f32 v35, v35\n\t"
        "v_exp_f32 v36, v36\n\t"
        "v_exp_f32 v37, v37\n\t"
        "v_mfma_f32_32x32x16_bf16 v[16:31], v[120:123], v[112:115], v[80:95]\n\t"
        "v_exp_f32 v38, v38\n\t"
        "v_exp_f32 v39, v39\n\t"
        "v_exp_f32 v40, v40\n\t"
        "v_exp_f32 v41, v41\n\t"
        "v_smfmac_f32_32x32x32_bf16 v[64:79], v[132:135], v[32:39], v140\n\t"
        "v_exp_f32 v42, v42\n\t"
        "v_exp_f32 v43, v43\n\t"
        "v_exp_f32 v44, v44\n\t"
        "v_exp_f32 v45, v45\n\t"
        "v_mfma_f32_32x32x16_bf16 v[192:207], v[220:223], v[212:215], v[192:207]\n\t"
        "buffer_load_dwordx4 v[220:223], v142, %[rs], %[sw] offen offset:1024 sc1\n\t"
        "buffer_load_dwordx4 v[212:215], v142, %[rs], %[sa] offen offset:1024 sc1\n\t"
        "v_exp_f32 v46, v46\n\t"
        "v_exp_f32 v47, v47\n\t"
        "v_exp_f32 v16, v16\n\t"
        "v_exp_f32 v17, v17\n\t"
        "v_smfmac_f32_32x32x32_bf16 v[64:79], v[136:139], v[40:47], v140\n\t"
        "s_mov_b64 exec, %[vm]\n\t"
        "ds_read_b128 v[132:135], v141 offset:29696\n\t"
        "ds_read_b128 v[136:139], v141 offset:29952\n\t"
        "s_mov_b64 exec, -1\n\t"
        "v_exp_f32 v18, v18\n\t"
        "v_exp_f32 v19, v19\n\t"
        "v_exp_f32 v20, v20\n\t"
        "v_exp_f32 v21, v21\n\t"
        "v_mfma_f32_32x32x16_bf16 v[32:47], v[120:123], v[116:119], v[80:95]\n\t"
        "v_exp_f32 v22, v22\n\t"
        "v_exp_f32 v23, v23\n\t"
        "v_exp_f32 v24, v24\n\t"
        "v_exp_f32 v25, v25\n\t"
        "v_smfmac_f32_32x32x32_bf16 v[48:63], v[124:127], v[16:23], v140\n\t"
        "ds_read_b128 v[120:123], v141 offset:28672\n\t"
        "s_waitcnt vmcnt(4)\n\t"
        "v_exp_f32 v26, v26\n\t"
        "v_exp_f32 v27, v27\n\t"
        "v_exp_f32 v28, v28\n\t"
        "v_exp_f32 v29, v29\n\t"
        "v_mfma_f32_32x32x16_bf16 v[144:159], v[232:235], v[224:227], v[144:159]\n\t"
        "v_exp_f32 v30, v30\n\t"
        "v_exp_f32 v31, v31\n\t"
        "v_exp_f32 v32, v32\n\t"
        "v_exp_f32 v33, v33\n\t"
        "v_smfmac_f32_32x32x32_bf16 v[48:63], v[128:131], v[24:31], v140\n\t"
        "s_waitcnt lgkmcnt(0)\n\t"
        "v_exp_f32 v34, v34\n\t"
        "v_exp_f32 v35, v35\n\t"
        "v_exp_f32 v36, v36\n\t"
        "v_exp_f32 v37, v37\n\t"
        "v_mfma_f32_32x32x16_bf16 v[16:31], v[120:123], v[112:115], v[80:95]\n\t"
        "v_exp_f32 v38, v38\n\t"
        "v_exp_f32 v39, v39\n\t"
        "v_exp_f32 v40, v40\n\t"
        "v_exp_f32 v41, v41\n\t"
        "v_smfmac_f32_32x32x32_bf16 v[64:79], v[124:127], v[32:39], v140\n\t"
        "v_exp_f32 v42, v42\n\t"
        "v_exp_f32 v43, v43\n\t"
        "v_exp_f32 v44, v44\n\t"
        "v_exp_f32 v45, v45\n\t"
        "v_mfma_f32_32x32x16_bf16 v[160:175], v[236:239], v[224:227], v[160:175]\n\t"
        "buffer_load_dwordx4 v[224:227], v142, %[rs], %[sa] offen offset:2048 sc1\n\t"
        "v_exp_f32 v46, v46\n\t"
        "v_exp_f32 v47, v47\n\t"
        "v_exp_f32 v16, v16\n\t"
        "v_exp_f32 v17, v17\n\t"
        "v_smfmac_f32_32x32x32_bf16 v[64:79], v[128:131], v[40:47], v140\n\t"
        "s_mov_b64 exec, %[vm]\n\t"
        "ds_read_b128 v[124:127], v141 offset:33792\n\t"
        "ds_read_b128 v[128:131], v141 offset:34048\n\t"
        "s_mov_b64 exec, -1\n\t"
        "v_exp_f32 v18, v18\n\t"
        "v_exp_f32 v19, v19\n\t"
        "v_exp_f32 v20, v20\n\t"
        "v_exp_f32 v21, v21\n\t"
        "v_mfma_f32_32x32x16_bf16 v[32:47], v[120:123], v[116:119], v[80:95]\n\t"
        "v_exp_f32 v22, v22\n\t"
        "v_exp_f32 v23, v23\n\t"
        "v_exp_f32 v24, v24\n\t"
        "v_exp_f32 v25, v25\n\t"
        "v_smfmac_f32_32x32x32_bf16 v[48:63], v[132:135], v[16:23], v140\n\t"
        "ds_read_b128 v[120:123], v141 offset:32768\n\t"
        "v_exp_f32 v26, v26\n\t"
        "v_exp_f32 v27, v27\n\t"
        "v_exp_f32 v28, v28\n\t"
        "v_exp_f32 v29, v29\n\t"
        "v_mfma_f32_32x32x16_bf16 v[176:191], v[232:235], v[228:231], v[176:191]\n\t"
        "buffer_load_dwordx4 v[232:235], v142, %[rs], %[sw] offen offset:2048 sc1\n\t"
        "v_exp_f32 v30, v30\n\t"
        "v_exp_f32 v31, v31\n\t"
        "v_exp_f32 v32, v32\n\t"
        "v_exp_f32 v33, v33\n\t"
        "v_smfmac_f32_32x32x32_bf16 v[48:63], v[136:139], v[24:31], v140\n\t"
        "s_waitcnt lgkmcnt(0)\n\t"
        "v_exp_f32 v34, v34\n\t"
        "v_exp_f32 v35, v35\n\t"
        "v_exp_f32 v36, v36\n\t"
        "v_exp_f32 v37, v37\n\t"
        "v_mfma_f32_32x32x16_bf16 v[16:31], v[120:123], v[112:115], v[80:95]\n\t"
        "v_exp_f32 v38, v38\n\t"
        "v_exp_f32 v39, v39\n\t"
        "v_exp_f32 v40, v40\n\t"
        "v_exp_f32 v41, v41\n\t"
        "v_smfmac_f32_32x32x32_bf16 v[64:79], v[132:135], v[32:39], v140\n\t"
        "v_exp_f32 v42, v42\n\t"
        "v_exp_f32 v43, v43\n\t"
        "v_exp_f32 v44, v44\n\t"
        "v_exp_f32 v45, v45\n\t"
        "v_mfma_f32_32x32x16_bf16 v[192:207], v[236:239], v[228:231], v[192:207]\n\t"
        "buffer_load_dwordx4 v[236:239], v142, %[rs], %[sw] offen offset:3072 sc1\n\t"
        "buffer_load_dwordx4 v[228:231], v142, %[rs], %[sa] offen offset:3072 sc1\n\t"
        "s_addk_i32 %[sa], 0x1000\n\t"
        "s_addk_i32 %[sw], 0x1000\n\t"
        "v_exp_f32 v46, v46\n\t"
        "v_exp_f32 v47, v47\n\t"
        "v_add_u32 v141, 0x8000, v141\n\t"
        "v_exp_f32 v16, v16\n\t"
        "v_exp_f32 v17, v17\n\t"
        "v_smfmac_f32_32x32x32_bf16 v[64:79], v[136:139], v[40:47], v140\n\t"
        "s_mov_b64 exec, %[vm]\n\t"
        "ds_read_b128 v[132:135], v141 offset:5120\n\t"
        "ds_read_b128 v[136:139], v141 offset:5376\n\t"
        "s_mov_b64 exec, -1\n\t"
        "v_exp_f32 v18, v18\n\t"
        "v_exp_f32 v19, v19\n\t"
        "v_exp_f32 v20, v20\n\t"
        "v_exp_f32 v21, v21\n\t"
        "v_mfma_f32_32x32x16_bf16 v[32:47], v[120:123], v[116:119], v[80:95]\n\t"
        "v_exp_f32 v22, v22\n\t"
        "v_exp_f32 v23, v23\n\t"
        "v_exp_f32 v24, v24\n\t"
        "v_exp_f32 v25, v25\n\t"
        "v_smfmac_f32_32x32x32_bf16 v[48:63], v[124:127], v[16:23], v140\n\t"
        "ds_read_b128 v[120:123], v141 offset:4096\n\t"
        "s_waitcnt vmcnt(4)\n\t"
        "v_exp_f32 v26, v26\n\t"
        "v_exp_f32 v27, v27\n\t"
        "v_exp_f32 v28, v28\n\t"
        "v_exp_f32 v29, v29\n\t"
        "v_mfma_f32_32x32x16_bf16 v[144:159], v[216:219], v[208:211], v[144:159]\n\t"
        "v_exp_f32 v30, v30\n\t"
        "v_exp_f32 v31, v31\n\t"
        "v_exp_f32 v32, v32\n\t"
        "v_exp_f32 v33, v33\n\t"
        "v_smfmac_f32_32x32x32_bf16 v[48:63], v[128:131], v[24:31], v140\n\t"
        "s_waitcnt lgkmcnt(0)\n\t"
        "v_exp_f32 v34, v34\n\t"
        "v_exp_f32 v35, v35\n\t"
        "v_exp_f32 v36, v36\n\t"
        "v_exp_f32 v37, v37\n\t"
        "v_mfma_f32_32x32x16_bf16 v[16:31], v[120:123], v[112:115], v[80:95]\n\t"
        "v_exp_f32 v38, v38\n\t"
        "v_exp_f32 v39, v39\n\t"
        "v_exp_f32 v40, v40\n\t"
        "v_exp_f32 v41, v41\n\t"
        "v_smfmac_f32_32x32x32_bf16 v[64:79], v[124:127], v[32:39], v140\n\t"
        "v_exp_f32 v42, v42\n\t"
        "v_exp_f32 v43, v43\n\t"
        "v_exp_f32 v44, v44\n\t"
        "v_exp_f32 v45, v45\n\t"
        "v_mfma_f32_32x32x16_bf16 v[160:175], v[220:223], v[208:211], v[160:175]\n\t"
        "buffer_load_dwordx4 v[208:211], v142, %[rs], %[sa] offen offset:0 sc1\n\t"
        "v_exp_f32 v46, v46\n\t"
        "v_exp_f32 v47, v47\n\t"
        "v_exp_f32 v16, v16\n\t"
        "v_exp_f32 v17, v17\n\t"
        "v_smfmac_f32_32x32x32_bf16 v[64:79], v[128:131], v[40:47], v140\n\t"
        "s_mov_b64 exec, %[vm]\n\t"
        "ds_read_b128 v[124:127], v141 offset:9216\n\t"
        "ds_read_b128 v[128:131], v141 offset:9472\n\t"
        "s_mov_b64 exec, -1\n\t"
        "v_exp_f32 v18, v18\n\t"
        "v_exp_f32 v19, v19\n\t"
        "v_exp_f32 v20, v20\n\t"
        "v_exp_f32 v21, v21\n\t"
        "v_mfma_f32_32x32x16_bf16 v[32:47], v[120:123], v[116:119], v[80:95]\n\t"
        "v_exp_f32 v22, v22\n\t"
        "v_exp_f32 v23, v23\n\t"
        "v_exp_f32 v24, v24\n\t"
        "v_exp_f32 v25, v25\n\t"
        "v_smfmac_f32_32x32x32_bf16 v[48:63], v[132:135], v[16:23], v140\n\t"
        "ds_read_b128 v[120:123], v141 offset:8192\n\t"
        "v_exp_f32 v26, v26\n\t"
        "v_exp_f32 v27, v27\n\t"
        "v_exp_f32 v28, v28\n\t"
        "v_exp_f32 v29, v29\n\t"
        "v_mfma_f32_32x32x16_bf16 v[176:191], v[216:219], v[212:215], v[176:191]\n\t"
        "buffer_load_dwordx4 v[216:219], v142, %[rs], %[sw] offen offset:0 sc1\n\t"
        "v_exp_f32 v30, v30\n\t"
        "v_exp_f32 v31, v31\n\t"
        "v_exp_f32 v32, v32\n\t"
        "v_exp_f32 v33, v33\n\t"
        "v_smfmac_f32_32x32x32_bf16 v[48:63], v[136:139], v[24:31], v140\n\t"
        "s_waitcnt lgkmcnt(0)\n\t"
        "v_exp_f32 v34, v34\n\t"
        "v_exp_f32 v35, v35\n\t"
        "v_exp_f32 v36, v36\n\t"
        "v_exp_f32 v37, v37\n\t"
        "v_mfma_f32_32x32x16_bf16 v[16:31], v[120:123], v[112:115], v[80:95]\n\t"
        "v_exp_f32 v38, v38\n\t"
        "v_exp_f32 v39, v39\n\t"
        "v_exp_f32 v40, v40\n\t"
        "v_exp_f32 v41, v41\n\t"
        "v_smfmac_f32_32x32x32_bf16 v[64:79], v[132:135], v[32:39], v140\n\t"
        "v_exp_f32 v42, v42\n\t"
        "v_exp_f32 v43, v43\n\t"
        "v_exp_f32 v44, v44\n\t"
        "v_exp_f32 v45, v45\n\t"
        "v_mfma_f32_32x32x16_bf16 v[192:207], v[220:223], v[212:215], v[192:207]\n\t"
        "buffer_load_dwordx4 v[220:223], v142, %[rs], %[sw] offen offset:1024 sc1\n\t"
        "buffer_load_dwordx4 v[212:215], v142, %[rs], %[sa] offen offset:1024 sc1\n\t"
        "v_exp_f32 v46, v46\n\t"
        "v_exp_f32 v47, v47\n\t"
        "v_exp_f32 v16, v16\n\t"
        "v_exp_f32 v17, v17\n\t"
        "v_smfmac_f32_32x32x32_bf16 v[64:79], v[136:139], v[40:47], v140\n\t"
        "s_mov_b64 exec, %[vm]\n\t"
        "ds_read_b128 v[132:135], v141 offset:13312\n\t"
        "ds_read_b128 v[136:139], v141 offset:13568\n\t"
        "s_mov_b64 exec, -1\n\t"
        "v_exp_f32 v18, v18\n\t"
        "v_exp_f32 v19, v19\n\t"
        "v_exp_f32 v20, v20\n\t"
        "v_exp_f32 v21, v21\n\t"
        "v_mfma_f32_32x32x16_bf16 v[32:47], v[120:123], v[116:119], v[80:95]\n\t"
        "v_exp_f32 v22, v22\n\t"
        "v_exp_f32 v23, v23\n\t"
        "v_exp_f32 v24, v24\n\t"
        "v_exp_f32 v25, v25\n\t"
        "v_smfmac_f32_32x32x32_bf16 v[48:63], v[124:127], v[16:23], v140\n\t"
        "ds_read_b128 v[120:123], v141 offset:12288\n\t"
        "s_waitcnt vmcnt(4)\n\t"
        "v_exp_f32 v26, v26\n\t"
        "v_exp_f32 v27, v27\n\t"
        "v_exp_f32 v28, v28\n\t"
        "v_exp_f32 v29, v29\n\t"
        "v_mfma_f32_32x32x16_bf16 v[144:159], v[232:235], v[224:227], v[144:159]\n\t"
        "v_exp_f32 v30, v30\n\t"
        "v_exp_f32 v31, v31\n\t"
        "v_exp_f32 v32, v32\n\t"
        "v_exp_f32 v33, v33\n\t"
        "v_smfmac_f32_32x32x32_bf16 v[48:63], v[128:131], v[24:31], v140\n\t"
        "s_waitcnt lgkmcnt(0)\n\t"
        "v_exp_f32 v34, v34\n\t"
        "v_exp_f32 v35, v35\n\t"
        "v_exp_f32 v36, v36\n\t"
        "v_exp_f32 v37, v37\n\t"
        "v_mfma_f32_32x32x16_bf16 v[16:31], v[120:123], v[112:115], v[80:95]\n\t"
        "v_exp_f32 v38, v38\n\t"
        "v_exp_f32 v39, v39\n\t"
        "v_exp_f32 v40, v40\n\t"
        "v_exp_f32 v41, v41\n\t"
        "v_smfmac_f32_32x32x32_bf16 v[64:79], v[124:127], v[32:39], v140\n\t"
        "v_exp_f32 v42, v42\n\t"
        "v_exp_f32 v43, v43\n\t"
        "v_exp_f32 v44, v44\n\t"
        "v_exp_f32 v45, v45\n\t"
        "v_mfma_f32_32x32x16_bf16 v[160:175], v[236:239], v[224:227], v[160:175]\n\t"
        "buffer_load_dwordx4 v[224:227], v142, %[rs], %[sa] offen offset:2048 sc1\n\t"
        "v_exp_f32 v46, v46\n\t"
        "v_exp_f32 v47, v47\n\t"
        "v_exp_f32 v16, v16\n\t"
        "v_exp_f32 v17, v17\n\t"
        "v_smfmac_f32_32x32x32_bf16 v[64:79], v[128:131], v[40:47], v140\n\t"
        "s_mov_b64 exec, %[vm]\n\t"
        "ds_read_b128 v[124:127], v141 offset:17408\n\t"
        "ds_read_b128 v[128:131], v141 offset:17664\n\t"
        "s_mov_b64 exec, -1\n\t"
        "v_exp_f32 v18, v18\n\t"
        "v_exp_f32 v19, v19\n\t"
        "v_exp_f32 v20, v20\n\t"
        "v_exp_f32 v21, v21\n\t"
        "v_mfma_f32_32x32x16_bf16 v[32:47], v[120:123], v[116:119], v[80:95]\n\t"
        "v_exp_f32 v22, v22\n\t"
        "v_exp_f32 v23, v23\n\t"
        "v_exp_f32 v24, v24\n\t"
        "v_exp_f32 v25, v25\n\t"
        "v_smfmac_f32_32x32x32_bf16 v[48:63], v[132:135], v[16:23], v140\n\t"
        "ds_read_b128 v[120:123], v141 offset:16384\n\t"
        "v_exp_f32 v26, v26\n\t"
        "v_exp_f32 v27, v27\n\t"
        "v_exp_f32 v28, v28\n\t"
        "v_exp_f32 v29, v29\n\t"
        "v_mfma_f32_32x32x16_bf16 v[176:191], v[232:235], v[228:231], v[176:191]\n\t"
        "buffer_load_dwordx4 v[232:235], v142, %[rs], %[sw] offen offset:2048 sc1\n\t"
        "v_exp_f32 v30, v30\n\t"
        "v_exp_f32 v31, v31\n\t"
        "v_exp_f32 v32, v32\n\t"
        "v_exp_f32 v33, v33\n\t"
        "v_smfmac_f32_32x32x32_bf16 v[48:63], v[136:139], v[24:31], v140\n\t"
        "s_waitcnt lgkmcnt(0)\n\t"
        "v_exp_f32 v34, v34\n\t"
        "v_exp_f32 v35, v35\n\t"
        "v_exp_f32 v36, v36\n\t"
        "v_exp_f32 v37, v37\n\t"
        "v_mfma_f32_32x32x16_bf16 v[16:31], v[120:123], v[112:115], v[80:95]\n\t"
        "v_exp_f32 v38, v38\n\t"
        "v_exp_f32 v39, v39\n\t"
        "v_exp_f32 v40, v40\n\t"
        "v_exp_f32 v41, v41\n\t"
        "v_smfmac_f32_32x32x32_bf16 v[64:79], v[132:135], v[32:39], v140\n\t"
        "v_exp_f32 v42, v42\n\t"
        "v_exp_f32 v43, v43\n\t"
        "v_exp_f32 v44, v44\n\t"
        "v_exp_f32 v45, v45\n\t"
        "v_mfma_f32_32x32x16_bf16 v[192:207], v[236:239], v[228:231], v[192:207]\n\t"
        "buffer_load_dwordx4 v[236:239], v142, %[rs], %[sw] offen offset:3072 sc1\n\t"
        "buffer_load_dwordx4 v[228:231], v142, %[rs], %[sa] offen offset:3072 sc1\n\t"
        "s_addk_i32 %[sa], 0x1000\n\t"
        "s_addk_i32 %[sw], 0x1000\n\t"
        "v_exp_f32 v46, v46\n\t"
        "v_exp_f32 v47, v47\n\t"
        "s_mov_b32 %[sa], %[swo]\n\t"
        "s_add_u32 %[sw], %[swo], 0x1000\n\t"
        "v_exp_f32 v16, v16\n\t"
        "v_exp_f32 v17, v17\n\t"
        "v_smfmac_f32_32x32x32_bf16 v[64:79], v[136:139], v[40:47], v140\n\t"
        "s_mov_b64 exec, %[vm]\n\t"
        "ds_read_b128 v[132:135], v141 offset:21504\n\t"
        "ds_read_b128 v[136:139], v141 offset:21760\n\t"
        "s_mov_b64 exec, -1\n\t"
        "v_exp_f32 v18, v18\n\t"
        "v_exp_f32 v19, v19\n\t"
        "v_exp_f32 v20, v20\n\t"
        "v_exp_f32 v21, v21\n\t"
        "v_mfma_f32_32x32x16_bf16 v[32:47], v[120:123], v[116:119], v[80:95]\n\t"
        "v_exp_f32 v22, v22\n\t"
        "v_exp_f32 v23, v23\n\t"
        "v_exp_f32 v24, v24\n\t"
        "v_exp_f32 v25, v25\n\t"
        "v_smfmac_f32_32x32x32_bf16 v[48:63], v[124:127], v[16:23], v140\n\t"
        "ds_read_b128 v[120:123], v141 offset:20480\n\t"
        "s_waitcnt vmcnt(4)\n\t"
        "v_exp_f32 v26, v26\n\t"
        "v_exp_f32 v27, v27\n\t"
        "v_exp_f32 v28, v28\n\t"
        "v_exp_f32 v29, v29\n\t"
        "v_mfma_f32_32x32x16_bf16 v[144:159], v[216:219], v[208:211], v[144:159]\n\t"
        "v_exp_f32 v30, v30\n\t"
        "v_exp_f32 v31, v31\n\t"
        "v_exp_f32 v32, v32\n\t"
        "v_exp_f32 v33, v33\n\t"
        "v_smfmac_f32_32x32x32_bf16 v[48:63], v[128:131], v[24:31], v140\n\t"
        "s_waitcnt lgkmcnt(0)\n\t"
        "v_exp_f32 v34, v34\n\t"
        "v_exp_f32 v35, v35\n\t"
        "v_exp_f32 v36, v36\n\t"
        "v_exp_f32 v37, v37\n\t"
        "v_mfma_f32_32x32x16_bf16 v[16:31], v[120:123], v[112:115], v[80:95]\n\t"
        "v_exp_f32 v38, v38\n\t"
        "v_exp_f32 v39, v39\n\t"
        "v_exp_f32 v40, v40\n\t"
        "v_exp_f32 v41, v41\n\t"
        "v_smfmac_f32_32x32x32_bf16 v[64:79], v[124:127], v[32:39], v140\n\t"
        "v_exp_f32 v42, v42\n\t"
        "v_exp_f32 v43, v43\n\t"
        "v_exp_f32 v44, v44\n\t"
        "v_exp_f32 v45, v45\n\t"
        "v_mfma_f32_32x32x16_bf16 v[160:175], v[220:223], v[208:211], v[160:175]\n\t"
        "buffer_load_dwordx4 v[208:211], v142, %[rs], %[sa] offen offset:0 sc1\n\t"
        "v_exp_f32 v46, v46\n\t"
        "v_exp_f32 v47, v47\n\t"
        "v_exp_f32 v16, v16\n\t"
        "v_exp_f32 v17, v17\n\t"
        "v_smfmac_f32_32x32x32_bf16 v[64:79], v[128:131], v[40:47], v140\n\t"
        "s_mov_b64 exec, %[vm]\n\t"
        "ds_read_b128 v[124:127], v141 offset:25600\n\t"
        "ds_read_b128 v[128:131], v141 offset:25856\n\t"
        "s_mov_b64 exec, -1\n\t"
        "v_exp_f32 v18, v18\n\t"
        "v_exp_f32 v19, v19\n\t"
        "v_exp_f32 v20, v20\n\t"
        "v_exp_f32 v21, v21\n\t"
        "v_mfma_f32_32x32x16_bf16 v[32:47], v[120:123], v[116:119], v[80:95]\n\t"
        "v_exp_f32 v22, v22\n\t"
        "v_exp_f32 v23, v23\n\t"
        "v_exp_f32 v24, v24\n\t"
        "v_exp_f32 v25, v25\n\t"
        "v_smfmac_f32_32x32x32_bf16 v[48:63], v[132:135], v[16:23], v140\n\t"
        "ds_read_b128 v[120:123], v141 offset:24576\n\t"
        "v_exp_f32 v26, v26\n\t"
        "v_exp_f32 v27, v27\n\t"
        "v_exp_f32 v28, v28\n\t"
        "v_exp_f32 v29, v29\n\t"
        "v_mfma_f32_32x32x16_bf16 v[176:191], v[216:219], v[212:215], v[176:191]\n\t"
        "buffer_load_dwordx4 v[216:219], v142, %[rs], %[sw] offen offset:0 sc1\n\t"
        "v_exp_f32 v30, v30\n\t"
        "v_exp_f32 v31, v31\n\t"
        "v_exp_f32 v32, v32\n\t"
        "v_exp_f32 v33, v33\n\t"
        "v_smfmac_f32_32x32x32_bf16 v[48:63], v[136:139], v[24:31], v140\n\t"
        "s_waitcnt lgkmcnt(0)\n\t"
        "v_exp_f32 v34, v34\n\t"
        "v_exp_f32 v35, v35\n\t"
        "v_exp_f32 v36, v36\n\t"
        "v_exp_f32 v37, v37\n\t"
        "v_mfma_f32_32x32x16_bf16 v[16:31], v[120:123], v[112:115], v[80:95]\n\t"
        "v_exp_f32 v38, v38\n\t"
        "v_exp_f32 v39, v39\n\t"
        "v_exp_f32 v40, v40\n\t"
        "v_exp_f32 v41, v41\n\t"
        "v_smfmac_f32_32x32x32_bf16 v[64:79], v[132:135], v[32:39], v140\n\t"
        "v_exp_f32 v42, v42\n\t"
        "v_exp_f32 v43, v43\n\t"
        "v_exp_f32 v44, v44\n\t"
        "v_exp_f32 v45, v45\n\t"
        "v_mfma_f32_32x32x16_bf16 v[192:207], v[220:223], v[212:215], v[192:207]\n\t"
        "buffer_load_dwordx4 v[220:223], v142, %[rs], %[sw] offen offset:1024 sc1\n\t"
        "buffer_load_dwordx4 v[212:215], v142, %[rs], %[sa] offen offset:1024 sc1\n\t"
        "v_exp_f32 v46, v46\n\t"
        "v_exp_f32 v47, v47\n\t"
        "s_cmp_lg_u32 %[kl], %[l0]\n\t"
        "s_cbranch_scc1 Lyo_%=\n\t"
        "s_setprio 0\n\t"
        "Lyo_%=:\n\t"
        "v_exp_f32 v16, v16\n\t"
        "v_exp_f32 v17, v17\n\t"
        "v_smfmac_f32_32x32x32_bf16 v[64:79], v[136:139], v[40:47], v140\n\t"
        "s_mov_b64 exec, %[vm]\n\t"
        "ds_read_b128 v[132:135], v141 offset:29696\n\t"
        "ds_read_b128 v[136:139], v141 offset:29952\n\t"
        "s_mov_b64 exec, -1\n\t"
        "v_exp_f32 v18, v18\n\t"
        "v_exp_f32 v19, v19\n\t"
        "v_exp_f32 v20, v20\n\t"
        "v_exp_f32 v21, v21\n\t"
        "v_mfma_f32_32x32x16_bf16 v[32:47], v[120:123], v[116:119], v[80:95]\n\t"
        "v_exp_f32 v22, v22\n\t"
        "v_exp_f32 v23, v23\n\t"
        "v_exp_f32 v24, v24\n\t"
        "v_exp_f32 v25, v25\n\t"
        "v_smfmac_f32_32x32x32_bf16 v[48:63], v[124:127], v[16:23], v140\n\t"
        "ds_read_b128 v[120:123], v141 offset:28672\n\t"
        "s_waitcnt vmcnt(4)\n\t"
        "v_exp_f32 v26, v26\n\t"
        "v_exp_f32 v27, v27\n\t"
        "v_exp_f32 v28, v28\n\t"
        "v_exp_f32 v29, v29\n\t"
        "v_mfma_f32_32x32x16_bf16 v[144:159], v[232:235], v[224:227], v[144:159]\n\t"
        "v_exp_f32 v30, v30\n\t"
        "v_exp_f32 v31, v31\n\t"
        "v_exp_f32 v32, v32\n\t"
        "v_exp_f32 v33, v33\n\t"
        "v_smfmac_f32_32x32x32_bf16 v[48:63], v[128:131], v[24:31], v140\n\t"
        "s_waitcnt lgkmcnt(0)\n\t"
        "v_exp_f32 v34, v34\n\t"
        "v_exp_f32 v35, v35\n\t"
        "v_exp_f32 v36, v36\n\t"
        "v_exp_f32 v37, v37\n\t"
        "v_mfma_f32_32x32x16_bf16 v[16:31], v[120:123], v[112:115], v[80:95]\n\t"
        "v_exp_f32 v38, v38\n\t"
        "v_exp_f32 v39, v39\n\t"
        "v_exp_f32 v40, v40\n\t"
        "v_exp_f32 v41, v41\n\t"
        "v_smfmac_f32_32x32x32_bf16 v[64:79], v[124:127], v[32:39], v140\n\t"
        "v_exp_f32 v42, v42\n\t"
        "v_exp_f32 v43, v43\n\t"
        "v_exp_f32 v44, v44\n\t"
        "v_exp_f32 v45, v45\n\t"
        "v_mfma_f32_32x32x16_bf16 v[160:175], v[236:239], v[224:227], v[160:175]\n\t"
        "buffer_load_dwordx4 v[224:227], v142, %[rs], %[sa] offen offset:2048 sc1\n\t"
        "v_exp_f32 v46, v46\n\t"
        "v_exp_f32 v47, v47\n\t"
        "v_exp_f32 v16, v16\n\t"
        "v_exp_f32 v17, v17\n\t"
        "v_smfmac_f32_32x32x32_bf16 v[64:79], v[128:131], v[40:47], v140\n\t"
        "s_mov_b64 exec, %[vm]\n\t"
        "ds_read_b128 v[124:127], v141 offset:33792\n\t"
        "ds_read_b128 v[128:131], v141 offset:34048\n\t"
        "s_mov_b64 exec, -1\n\t"
        "v_exp_f32 v18, v18\n\t"
        "v_exp_f32 v19, v19\n\t"
        "v_exp_f32 v20, v20\n\t"
        "v_exp_f32 v21, v21\n\t"
        "v_mfma_f32_32x32x16_bf16 v[32:47], v[120:123], v[116:119], v[80:95]\n\t"
        "v_exp_f32 v22, v22\n\t"
        "v_exp_f32 v23, v23\n\t"
        "v_exp_f32 v24, v24\n\t"
        "v_exp_f32 v25, v25\n\t"
        "v_smfmac_f32_32x32x32_bf16 v[48:63], v[132:135], v[16:23], v140\n\t"
        "ds_read_b128 v[120:123], v141 offset:32768\n\t"
        "v_exp_f32 v26, v26\n\t"
        "v_exp_f32 v27, v27\n\t"
        "v_exp_f32 v28, v28\n\t"
        "v_exp_f32 v29, v29\n\t"
        "v_mfma_f32_32x32x16_bf16 v[176:191], v[232:235], v[228:231], v[176:191]\n\t"
        "buffer_load_dwordx4 v[232:235], v142, %[rs], %[sw] offen offset:2048 sc1\n\t"
        "v_exp_f32 v30, v30\n\t"
        "v_exp_f32 v31, v31\n\t"
        "v_exp_f32 v32, v32\n\t"
        "v_exp_f32 v33, v33\n\t"
        "v_smfmac_f32_32x32x32_bf16 v[48:63], v[136:139], v[24:31], v140\n\t"
        "s_waitcnt lgkmcnt(0)\n\t"
        "v_exp_f32 v34, v34\n\t"
        "v_exp_f32 v35, v35\n\t"
        "v_exp_f32 v36, v36\n\t"
        "v_exp_f32 v37, v37\n\t"
        "v_mfma_f32_32x32x16_bf16 v[16:31], v[120:123], v[112:115], v[80:95]\n\t"
        "v_exp_f32 v38, v38\n\t"
        "v_exp_f32 v39, v39\n\t"
        "v_exp_f32 v40, v40\n\t"
        "v_exp_f32 v41, v41\n\t"
        "v_smfmac_f32_32x32x32_bf16 v[64:79], v[132:135], v[32:39], v140\n\t"
        "v_exp_f32 v42, v42\n\t"
        "v_exp_f32 v43, v43\n\t"
        "v_exp_f32 v44, v44\n\t"
        "v_exp_f32 v45, v45\n\t"
        "v_mfma_f32_32x32x16_bf16 v[192:207], v[236:239], v[228:231], v[192:207]\n\t"
        "buffer_load_dwordx4 v[236:239], v142, %[rs], %[sw] offen offset:3072 sc1\n\t"
        "buffer_load_dwordx4 v[228:231], v142, %[rs], %[sa] offen offset:3072 sc1\n\t"
        "s_addk_i32 %[sa], 0x1000\n\t"
        "s_addk_i32 %[sw], 0x1000\n\t"
        "v_exp_f32 v46, v46\n\t"
        "v_exp_f32 v47, v47\n\t"
        "v_add_u32 v141, %[mw], v142\n\t"
        "s_nop 0\n\t"
        "v_smfmac_f32_32x32x32_bf16 v[64:79], v[136:139], v[40:47], v140\n\t"
        "ds_write_b128 v141, v[48:51]\n\t"
        "ds_write_b128 v141, v[52:55] offset:1024\n\t"
        "ds_write_b32 v141, v56 offset:2048\n\t"
        "v_add_u32 v141, %[mr], v142\n\t"
        "s_mov_b32 %[cnt], 0x67800000\n\t"
        "s_mov_b32 %[stg], 0x3f000000\n\t"
        "s_mov_b32 %[t32], 0x7b800000\n\t"
        "s_waitcnt lgkmcnt(0)\n\t"
        "s_barrier\n\t"
        "ds_read_b32 v24, v141 offset:2048\n\t"
        "ds_read_b128 v[16:19], v141\n\t"
        "ds_read_b128 v[20:23], v141 offset:1024\n\t"
        "s_nop 2\n\t"
        "s_waitcnt lgkmcnt(2)\n\t"
        "v_add_f32 v72, v72, v24\n\t"
        "s_waitcnt lgkmcnt(1)\n\t"
        "v_add_f32 v64, v64, v16\n\t"
        "v_add_f32 v65, v65, v17\n\t"
        "v_add_f32 v66, v66, v18\n\t"
        "v_add_f32 v67, v67, v19\n\t"
        "v_cmp_gt_f32 vcc, %[cnt], v72\n\t"
        "v_cmp_lt_f32 %[t64], %[stg], v72\n\t"
        "s_waitcnt lgkmcnt(0)\n\t"
        "v_add_f32 v68, v68, v20\n\t"
        "v_add_f32 v69, v69, v21\n\t"
        "v_add_f32 v70, v70, v22\n\t"
        "v_add_f32 v71, v71, v23\n\t"
        "v_add_f32 v16, abs(v64), abs(v65)\n\t"
        "v_add_f32 v17, abs(v66), abs(v67)\n\t"
        "v_add_f32 v18, abs(v68), abs(v69)\n\t"
        "v_add_f32 v19, abs(v70), abs(v71)\n\t"
        "s_and_b64 %[t64], %[t64], vcc\n\t"
        "v_add_f32 v16, v16, v17\n\t"
        "v_add_f32 v18, v18, v19\n\t"
        "v_add_f32 v16, v16, v18\n\t"
        "v_cmp_gt_f32 vcc, %[t32], v16\n\t"
        "s_and_b64 %[t64], %[t64], vcc\n\t"
        "s_cmp_eq_u64 %[t64], -1\n\t"
        "s_cbranch_scc0 Luns_%=\n\t"
        "Lsafe_%=:\n\t"
        : "+{v[48:63]}"(o0), "+{v[64:79]}"(o1), "+{v[124:139]}"(vv), [cnt] "=&s"(cnt), [stg] "=&s"(stg), [sa] "+s"(sa), [sw] "+s"(sw), [t64] "=&s"(t64), [t32] "=&s"(t32),
          "+{v[144:159]}"(c00), "+{v[160:175]}"(c01), "+{v[176:191]}"(c10), "+{v[192:207]}"(c11), "=&{v[208:223]}"(ga), "=&{v[224:239]}"(gb), [kvs] "+s"(kvs),
          "=&{v[116:119]}"(q1), "=&{v[96:111]}"(xa_), "=&{v[240:255]}"(xb_), "=&{v13}"(go), "=&{v14}"(g0), "=&{v15}"(g1)
        : "{v142}"(goff), [vm] "s"(0x0000FFFF0000FFFFull), [rs] "s"(ws), [lcnt] "s"(lds0 + LDS_CNT), [taglo] "s"(taglo), [taghi] "s"(taghi), [fh] "s"(WS_FH + blockIdx.x * FHS), [fhb] "s"(__builtin_amdgcn_readfirstlane(WS_FH + (w & 3) * 64 * FHS)),
          [sgo] "s"(WS_G + (qrt + kh) * 128), [sga] "s"(WS_G + rp * 256), [kvl] "s"(lds0 + KV1 * 4096 + w * 1024), [mw] "s"(lds0 + LDS_MRG + w * 3072), [mr] "s"(lds0 + LDS_MRG + (w ^ 4) * 3072),
          [swo] "s"(WS_W + (unsigned)(cp * NKS + w * 4) * 2048), [ql] "s"(__builtin_amdgcn_readfirstlane(lds0 + LDS_Q + (w & 3) * 2048 + kh * 1024)), [l0] "s"(lds0), [eo] "s"(blockIdx.x + w), [we] "i"(WS_E), [oob] "i"(WS_END), [kl] "s"(lds0 + kh * 2048)
        : "memory", "scc", "vcc", "v16", "v17", "v18", "v19", "v20", "v21", "v22", "v23", "v24", "v25", "v26", "v27", "v28", "v29", "v30", "v31", "v32", "v33", "v34", "v35", "v36", "v37", "v38", "v39", "v40", "v41", "v42", "v43", "v44", "v45", "v46", "v47", "v80", "v81", "v82", "v83", "v84", "v85", "v86", "v87", "v88", "v89", "v90", "v91", "v92", "v93", "v94", "v95", "v112", "v113", "v114", "v115", "v120", "v121", "v122", "v123", "v140", "v141");

    }
    if (__builtin_expect(t64 != ~0ull, 0)) {
        const unsigned kb = WS_KV + (unsigned)head * 131072;
        float m = -INFINITY;
#pragma unroll 1
        for (int t = 0; t < 64; ++t) m = fmaxf(m, max16(MFMA(ld16(ws, goff, kb + t * 2048), q1, f32x16{})));
        const f32x16 nm = splat16(-halfmax(m));
        o1 = f32x16{};
        const u32x4 one = {0x3F803F80u, 0x3F803F80u, 0x3F803F80u, 0x3F803F80u};
#pragma unroll 1
        for (int t = 0; t < 64; ++t) {
            f32x16 s = MFMA(ld16(ws, goff, kb + t * 2048), q1, nm);
#pragma unroll
            for (int i = 0; i < 16; ++i) s[i] = __builtin_amdgcn_exp2f(s[i]);
            const u32x4 va = ld16u(ws, goff & ~256u, kb + t * 2048 + 1024), vb = ld16u(ws, goff & ~256u, kb + t * 2048 + 1280);
            o1 = pv(__builtin_bit_cast(bf16x8, (goff & 256) ? one : va), s, 0, o1); o1 = pv(__builtin_bit_cast(bf16x8, (goff & 256) ? one : vb), s, 1, o1);
        }
        asm volatile("s_nop 11" : "+v"(o1));
    }
    const unsigned so = WS_A + (unsigned)(rp * NKS + w * 4) * 2048;
    asm volatile(
        "v_rcp_f32 v24, v72\n\t"
        "v_add_u32 v25, %[osl], v142\n\t"
        "v_mul_f32 v24, v13, v24\n\t"
        "v_mul_f32 v16, v64, v24\n\t"
        "v_mul_f32 v17, v65, v24\n\t"
        "v_mul_f32 v18, v66, v24\n\t"
        "v_mul_f32 v19, v67, v24\n\t"
        "v_mul_f32 v20, v68, v24\n\t"
        "v_mul_f32 v21, v69, v24\n\t"
        "v_mul_f32 v22, v70, v24\n\t"
        "v_mul_f32 v23, v71, v24\n\t"
        "v_cvt_pk_bf16_f32 v28, v16, v17\n\t"
        "v_cvt_pk_bf16_f32 v29, v18, v19\n\t"
        "v_cvt_pk_bf16_f32 v30, v20, v21\n\t"
        "v_cvt_pk_bf16_f32 v31, v22, v23\n\t"
        "v_min_u32 v28, -2, v28\n\t"
        "v_min_u32 v29, -2, v29\n\t"
        "v_min_u32 v30, -2, v30\n\t"
        "v_min_u32 v31, -2, v31\n\t"
        "buffer_store_dwordx4 v[28:31], v25, %[rs], 0 offen sc1\n\t"
        "v_sub_f32 v14, 1.0, v14\n\t"
        "v_sub_f32 v15, 1.0, v15\n\t"
        "v_add_u32 v16, %[rw], v142\n\t"
        "v_add_u32 v17, %[rr], v142\n\t"
        "v_add_u32 v18, 0x10000, v17\n\t"
        "s_mov_b32 %[cnt], 0x40000\n\t"
        "v_mfma_f32_32x32x16_bf16 v[144:159], v[104:107], v[96:99], v[144:159]\n\t"
        "v_mfma_f32_32x32x16_bf16 v[160:175], v[108:111], v[96:99], v[160:175]\n\t"
        "v_mfma_f32_32x32x16_bf16 v[176:191], v[104:107], v[100:103], v[176:191]\n\t"
        "v_mfma_f32_32x32x16_bf16 v[192:207], v[108:111], v[100:103], v[192:207]\n\t"
        "v_mfma_f32_32x32x16_bf16 v[144:159], v[248:251], v[240:243], v[144:159]\n\t"
        "v_mfma_f32_32x32x16_bf16 v[160:175], v[252:255], v[240:243], v[160:175]\n\t"
        "v_mfma_f32_32x32x16_bf16 v[176:191], v[248:251], v[244:247], v[176:191]\n\t"
        "v_mfma_f32_32x32x16_bf16 v[192:207], v[252:255], v[244:247], v[192:207]\n\t"
        "s_nop 8\n\t"
        "v_mul_f32 v144, v14, v144\n\t"
        "v_mul_f32 v145, v14, v145\n\t"
        "v_mul_f32 v146, v14, v146\n\t"
        "v_mul_f32 v147, v14, v147\n\t"
        "v_mul_f32 v148, v14, v148\n\t"
        "v_mul_f32 v149, v14, v149\n\t"
        "v_mul_f32 v150, v14, v150\n\t"
        "v_mul_f32 v151, v14, v151\n\t"
        "v_mul_f32 v152, v14, v152\n\t"
        "v_mul_f32 v153, v14, v153\n\t"
        "v_mul_f32 v154, v14, v154\n\t"
        "v_mul_f32 v155, v14, v155\n\t"
        "v_mul_f32 v156, v14, v156\n\t"
        "v_mul_f32 v157, v14, v157\n\t"
        "v_mul_f32 v158, v14, v158\n\t"
        "v_mul_f32 v159, v14, v159\n\t"
        "v_mul_f32 v160, v14, v160\n\t"
        "v_mul_f32 v161, v14, v161\n\t"
        "v_mul_f32 v162, v14, v162\n\t"
        "v_mul_f32 v163, v14, v163\n\t"
        "v_mul_f32 v164, v14, v164\n\t"
        "v_mul_f32 v165, v14, v165\n\t"
        "v_mul_f32 v166, v14, v166\n\t"
        "v_mul_f32 v167, v14, v167\n\t"
        "v_mul_f32 v168, v14, v168\n\t"
        "v_mul_f32 v169, v14, v169\n\t"
        "v_mul_f32 v170, v14, v170\n\t"
        "v_mul_f32 v171, v14, v171\n\t"
        "v_mul_f32 v172, v14, v172\n\t"
        "v_mul_f32 v173, v14, v173\n\t"
        "v_mul_f32 v174, v14, v174\n\t"
        "v_mul_f32 v175, v14, v175\n\t"
        "v_mul_f32 v176, v15, v176\n\t"
        "v_mul_f32 v177, v15, v177\n\t"
        "v_mul_f32 v178, v15, v178\n\t"
        "v_mul_f32 v179, v15, v179\n\t"
        "v_mul_f32 v180, v15, v180\n\t"
        "v_mul_f32 v181, v15, v181\n\t"
        "v_mul_f32 v182, v15, v182\n\t"
        "v_mul_f32 v183, v15, v183\n\t"
        "v_mul_f32 v184, v15, v184\n\t"
        "v_mul_f32 v185, v15, v185\n\t"
        "v_mul_f32 v186, v15, v186\n\t"
        "v_mul_f32 v187, v15, v187\n\t"
        "v_mul_f32 v188, v15, v188\n\t"
        "v_mul_f32 v189, v15, v189\n\t"
        "v_mul_f32 v190, v15, v190\n\t"
        "v_mul_f32 v191, v15, v191\n\t"
        "v_mul_f32 v192, v15, v192\n\t"
        "v_mul_f32 v193, v15, v193\n\t"
        "v_mul_f32 v194, v15, v194\n\t"
        "v_mul_f32 v195, v15, v195\n\t"
        "v_mul_f32 v196, v15, v196\n\t"
        "v_mul_f32 v197, v15, v197\n\t"
        "v_mul_f32 v198, v15, v198\n\t"
        "v_mul_f32 v199, v15, v199\n\t"
        "v_mul_f32 v200, v15, v200\n\t"
        "v_mul_f32 v201, v15, v201\n\t"
        "v_mul_f32 v202, v15, v202\n\t"
        "v_mul_f32 v203, v15, v203\n\t"
        "v_mul_f32 v204, v15, v204\n\t"
        "v_mul_f32 v205, v15, v205\n\t"
        "v_mul_f32 v206, v15, v206\n\t"
        "v_mul_f32 v207, v15, v207\n\t"
        "s_sleep 7\n\t"
        "Lop_%=:\n\t"
        "buffer_load_dwordx4 v[48:51], v142, %[rs], %[so0] offen offset:0 sc1\n\t"
        "buffer_load_dwordx4 v[52:55], v142, %[rs], %[so0] offen offset:1024 sc1\n\t"
        "buffer_load_dwordx4 v[56:59], v142, %[rs], %[so0] offen offset:2048 sc1\n\t"
        "buffer_load_dwordx4 v[60:63], v142, %[rs], %[so0] offen offset:3072 sc1\n\t"
        "buffer_load_dwordx4 v[64:67], v142, %[rs], %[so1] offen offset:0 sc1\n\t"
        "buffer_load_dwordx4 v[68:71], v142, %[rs], %[so1] offen offset:1024 sc1\n\t"
        "buffer_load_dwordx4 v[72:75], v142, %[rs], %[so1] offen offset:2048 sc1\n\t"
        "buffer_load_dwordx4 v[76:79], v142, %[rs], %[so1] offen offset:3072 sc1\n\t"
        "s_addk_i32 %[cnt], -1\n\t"
        "s_waitcnt vmcnt(7)\n\t"
        "v_max3_u32 v20, v48, v49, v50\n\t"
        "v_mov_b32 v21, v51\n\t"
        "s_waitcnt vmcnt(6)\n\t"
        "v_max3_u32 v20, v20, v52, v53\n\t"
        "v_max3_u32 v21, v21, v54, v55\n\t"
        "s_waitcnt vmcnt(5)\n\t"
        "v_max3_u32 v20, v20, v56, v57\n\t"
        "v_max3_u32 v21, v21, v58, v59\n\t"
        "s_waitcnt vmcnt(4)\n\t"
        "v_max3_u32 v20, v20, v60, v61\n\t"
        "v_max3_u32 v21, v21, v62, v63\n\t"
        "s_waitcnt vmcnt(3)\n\t"
        "v_max3_u32 v20, v20, v64, v65\n\t"
        "v_max3_u32 v21, v21, v66, v67\n\t"
        "s_waitcnt vmcnt(2)\n\t"
        "v_max3_u32 v20, v20, v68, v69\n\t"
        "v_max3_u32 v21, v21, v70, v71\n\t"
        "s_waitcnt vmcnt(1)\n\t"
        "v_max3_u32 v20, v20, v72, v73\n\t"
        "v_max3_u32 v21, v21, v74, v75\n\t"
        "s_waitcnt vmcnt(0)\n\t"
        "v_max3_u32 v20, v20, v76, v77\n\t"
        "v_max3_u32 v21, v21, v78, v79\n\t"
        "v_max_u32 v20, v20, v21\n\t"
        "v_cmp_eq_u32 vcc, -1, v20\n\t"
        "s_cmp_gt_i32 %[cnt], 0\n\t"
        "s_cselect_b64 vcc, vcc, 0\n\t"
        "s_cbranch_vccnz Lop_%=\n\t"
        "v_mfma_f32_32x32x16_bf16 v[144:159], v[208:211], v[48:51], v[144:159]\n\t"
        "v_mfma_f32_32x32x16_bf16 v[144:159], v[224:227], v[56:59], v[144:159]\n\t"
        "v_mfma_f32_32x32x16_bf16 v[144:159], v[216:219], v[64:67], v[144:159]\n\t"
        "v_mfma_f32_32x32x16_bf16 v[144:159], v[232:235], v[72:75], v[144:159]\n\t"
        "v_mfma_f32_32x32x16_bf16 v[160:175], v[212:215], v[48:51], v[160:175]\n\t"
        "v_mfma_f32_32x32x16_bf16 v[160:175], v[228:231], v[56:59], v[160:175]\n\t"
        "v_mfma_f32_32x32x16_bf16 v[160:175], v[220:223], v[64:67], v[160:175]\n\t"
        "v_mfma_f32_32x32x16_bf16 v[160:175], v[236:239], v[72:75], v[160:175]\n\t"
        "v_mfma_f32_32x32x16_bf16 v[176:191], v[208:211], v[52:55], v[176:191]\n\t"
        "s_nop 6\n\t"
        "ds_write_b128 v16, v[144:147] offset:0\n\t"
        "v_mfma_f32_32x32x16_bf16 v[176:191], v[224:227], v[60:63], v[176:191]\n\t"
        "ds_write_b128 v16, v[148:151] offset:1024\n\t"
        "v_mfma_f32_32x32x16_bf16 v[176:191], v[216:219], v[68:71], v[176:191]\n\t"
        "ds_write_b128 v16, v[152:155] offset:2048\n\t"
        "v_mfma_f32_32x32x16_bf16 v[176:191], v[232:235], v[76:79], v[176:191]\n\t"
        "ds_write_b128 v16, v[156:159] offset:3072\n\t"
        "v_mfma_f32_32x32x16_bf16 v[192:207], v[212:215], v[52:55], v[192:207]\n\t"
        "s_nop 1\n\t"
        "ds_write_b128 v16, v[160:163] offset:4096\n\t"
        "v_mfma_f32_32x32x16_bf16 v[192:207], v[228:231], v[60:63], v[192:207]\n\t"
        "ds_write_b128 v16, v[164:167] offset:5120\n\t"
        "v_mfma_f32_32x32x16_bf16 v[192:207], v[220:223], v[68:71], v[192:207]\n\t"
        "ds_write_b128 v16, v[168:171] offset:6144\n\t"
        "v_mfma_f32_32x32x16_bf16 v[192:207], v[236:239], v[76:79], v[192:207]\n\t"
        "ds_write_b128 v16, v[172:175] offset:7168\n\t"
        "s_nop 3\n\t"
        "ds_write_b128 v16, v[176:179] offset:8192\n\t"
        "ds_write_b128 v16, v[180:183] offset:9216\n\t"
        "ds_write_b128 v16, v[184:187] offset:10240\n\t"
        "ds_write_b128 v16, v[188:191] offset:11264\n\t"
        "s_nop 3\n\t"
        "ds_write_b128 v16, v[192:195] offset:12288\n\t"
        "ds_write_b128 v16, v[196:199] offset:13312\n\t"
        "ds_write_b128 v16, v[200:203] offset:14336\n\t"
        "ds_write_b128 v16, v[204:207] offset:15360\n\t"
        "s_waitcnt lgkmcnt(0)\n\t"
        "s_barrier\n\t"
        "ds_read_b128 v[32:35], v17 offset:0\n\t"
        "ds_read_b128 v[36:39], v17 offset:1024\n\t"
        "ds_read_b128 v[40:43], v17 offset:16384\n\t"
        "ds_read_b128 v[44:47], v17 offset:17408\n\t"
        "ds_read_b128 v[48:51], v17 offset:32768\n\t"
        "ds_read_b128 v[52:55], v17 offset:33792\n\t"
        "ds_read_b128 v[56:59], v17 offset:49152\n\t"
        "ds_read_b128 v[60:63], v17 offset:50176\n\t"
        "ds_read_b128 v[64:67], v18 offset:0\n\t"
        "ds_read_b128 v[68:71], v18 offset:1024\n\t"
        "ds_read_b128 v[72:75], v18 offset:16384\n\t"
        "ds_read_b128 v[76:79], v18 offset:17408\n\t"
        "ds_read_b128 v[80:83], v18 offset:32768\n\t"
        "ds_read_b128 v[84:87], v18 offset:33792\n\t"
        "ds_read_b128 v[88:91], v18 offset:49152\n\t"
        "ds_read_b128 v[92:95], v18 offset:50176\n\t"
        "s_waitcnt lgkmcnt(12)\n\t"
        "v_pk_add_f32 v[32:33], v[32:33], v[40:41]\n\t"
        "v_pk_add_f32 v[34:35], v[34:35], v[42:43]\n\t"
        "v_pk_add_f32 v[36:37], v[36:37], v[44:45]\n\t"
        "v_pk_add_f32 v[38:39], v[38:39], v[46:47]\n\t"
        "s_waitcnt lgkmcnt(10)\n\t"
        "v_pk_add_f32 v[32:33], v[32:33], v[48:49]\n\t"
        "v_pk_add_f32 v[34:35], v[34:35], v[50:51]\n\t"
        "v_pk_add_f32 v[36:37], v[36:37], v[52:53]\n\t"
        "v_pk_add_f32 v[38:39], v[38:39], v[54:55]\n\t"
        "s_waitcnt lgkmcnt(8)\n\t"
        "v_pk_add_f32 v[32:33], v[32:33], v[56:57]\n\t"
        "v_pk_add_f32 v[34:35], v[34:35], v[58:59]\n\t"
        "v_pk_add_f32 v[36:37], v[36:37], v[60:61]\n\t"
        "v_pk_add_f32 v[38:39], v[38:39], v[62:63]\n\t"
        "s_waitcnt lgkmcnt(6)\n\t"
        "v_pk_add_f32 v[32:33], v[32:33], v[64:65]\n\t"
        "v_pk_add_f32 v[34:35], v[34:35], v[66:67]\n\t"
        "v_pk_add_f32 v[36:37], v[36:37], v[68:69]\n\t"
        "v_pk_add_f32 v[38:39], v[38:39], v[70:71]\n\t"
        "s_waitcnt lgkmcnt(4)\n\t"
        "v_pk_add_f32 v[32:33], v[32:33], v[72:73]\n\t"
        "v_pk_add_f32 v[34:35], v[34:35], v[74:75]\n\t"
        "v_pk_add_f32 v[36:37], v[36:37], v[76:77]\n\t"
        "v_pk_add_f32 v[38:39], v[38:39], v[78:79]\n\t"
        "s_waitcnt lgkmcnt(2)\n\t"
        "v_pk_add_f32 v[32:33], v[32:33], v[80:81]\n\t"
        "v_pk_add_f32 v[34:35], v[34:35], v[82:83]\n\t"
        "v_pk_add_f32 v[36:37], v[36:37], v[84:85]\n\t"
        "v_pk_add_f32 v[38:39], v[38:39], v[86:87]\n\t"
        "s_waitcnt lgkmcnt(0)\n\t"
        "v_pk_add_f32 v[32:33], v[32:33], v[88:89]\n\t"
        "v_pk_add_f32 v[34:35], v[34:35], v[90:91]\n\t"
        "v_pk_add_f32 v[36:37], v[36:37], v[92:93]\n\t"
        "v_pk_add_f32 v[38:39], v[38:39], v[94:95]\n\t"
        "v_cvt_pk_bf16_f32 v28, v32, v33\n\t"
        "v_cvt_pk_bf16_f32 v29, v34, v35\n\t"
        "v_cvt_pk_bf16_f32 v30, v36, v37\n\t"
        "v_cvt_pk_bf16_f32 v31, v38, v39\n\t"
        "global_store_dwordx4 %[oo], v[28:31], %[out]\n\t"
        "Lgm_code_end:\n\t"
        : "+{v[144:159]}"(c00), "+{v[160:175]}"(c01), "+{v[176:191]}"(c10), "+{v[192:207]}"(c11), "+{v[64:79]}"(o1), "+{v14}"(g0), "+{v15}"(g1), [cnt] "=&s"(cnt)
        : "{v[208:223]}"(ga), "{v[224:239]}"(gb), "{v[96:111]}"(xa_), "{v[240:255]}"(xb_), "{v13}"(go), "{v142}"(goff), [rs] "s"(ws),
          [osl] "s"(oslot_u), [so0] "s"(so), [so1] "s"(so + 4096), [rw] "s"(lds0 + w * 16384), [rr] "s"(lds0 + w * 2048), [oo] "v"(ooff), [out] "s"(out)
        : "memory", "scc", "vcc", "v16", "v17", "v18", "v19", "v20", "v21", "v22", "v23", "v24", "v25", "v26", "v27", "v28", "v29", "v30", "v31", "v32", "v33", "v34", "v35", "v36", "v37", "v38", "v39", "v40", "v41", "v42", "v43", "v44", "v45", "v46", "v47", "v48", "v49", "v50", "v51", "v52", "v53", "v54", "v55", "v56", "v57", "v58", "v59", "v60", "v61", "v62", "v63", "v80", "v81", "v82", "v83", "v84", "v85", "v86", "v87", "v88", "v89", "v90", "v91", "v92", "v93", "v94", "v95");
}

extern "C" void kernel_launch(void* const* d_in, const int* in_sizes, int n_in, void* d_out, int out_size, void* d_ws, size_t ws_size, hipStream_t stream) {
    (void)hipFuncSetAttribute((const void*)k_fused, hipFuncAttributeMaxDynamicSharedMemorySize, 163840);
    k_fused<<<256, 512, 163840, stream>>>((const char*)d_in[0], (const char*)d_in[1], (const char*)d_in[2], (const char*)d_in[4], (const char*)d_in[5], (const char*)d_in[6], (char*)d_ws, (char*)d_out);
}
```
